```python
import torch

def _softmax(x, axis=-1):
    return torch.softmax(x, dim=axis)
def _gelu(x):
    return torch.nn.functional.gelu(x, approximate="tanh")
def _silu(x):
    return torch.nn.functional.silu(x)
def _sigmoid(x):
    return torch.sigmoid(x)
def _layernorm(x, axis=-1, eps=1e-5):
    m = x.mean(dim=axis, keepdim=True)
    v = x.var(dim=axis, keepdim=True, unbiased=False)
    return (x - m) / torch.sqrt(v + eps)
def _scatter_add(tgt, idx, src, axis=0):
    return tgt.clone().index_add_(axis, idx, src)
def _segment_sum(src, seg, n):
    out = torch.zeros((n,) + src.shape[1:], dtype=src.dtype)
    return out.index_add_(0, seg, src)
def _pad(x, pw):
    flat = tuple(v for pair in reversed(pw) for v in pair)
    return torch.nn.functional.pad(x, flat)

kernel_name = 'sequential_preLN__ssm_conv_s4__glu_gated_swiglu__prompt'

def setup_inputs():

    _w = (lambda a: (lambda t: t.to(torch.bfloat16) if t.is_floating_point() else t)(torch.as_tensor(a)))
    def _A_setup():
        B, L, D, N = 1, 2048, 576, 128
        A_re = -(lambda _x: torch.exp(torch.as_tensor(_x)))((lambda *s: torch.randn(*s, dtype=torch.bfloat16) * 6.4)(D, N))
        A_im = torch.pi * (lambda *s: torch.randn(*s, dtype=torch.bfloat16) * 0.05)(D, N)
        B_re = (lambda *s: torch.randn(*s, dtype=torch.bfloat16) * 0.05)(D, N)
        B_im = (lambda *s: torch.randn(*s, dtype=torch.bfloat16) * 0.05)(D, N)
        C_re = (lambda *s: torch.randn(*s, dtype=torch.bfloat16) * 0.05)(D, N)
        C_im = (lambda *s: torch.randn(*s, dtype=torch.bfloat16) * 0.05)(D, N)
        log_dt = (lambda *s: torch.randn(*s, dtype=torch.bfloat16) * 0.05)(D) * 0.5 - 4.0
        Dskip = (lambda *s: torch.randn(*s, dtype=torch.bfloat16) * 0.05)(D)
        return {
            "u": _w((lambda *s: torch.randn(*s, dtype=torch.bfloat16) * 0.05)(B, L, D)),
            "A_re": _w(A_re),
            "A_im": _w(A_im),
            "B_re": _w(B_re),
            "B_im": _w(B_im),
            "C_re": _w(C_re),
            "C_im": _w(C_im),
            "log_dt": _w(log_dt),
            "Dskip": _w(Dskip),
        }
    _A_in = _A_setup()

    def _B_setup():
        B, S, D, F = 1, 2048, 576, 4608
        return {
            "x": _w((lambda *s: torch.randn(*s, dtype=torch.bfloat16) * 0.05)(B, S, D)),
            "w1": _w((lambda *s: torch.randn(*s, dtype=torch.bfloat16) * 0.4)(D, F) * (2.0 / D) ** 0.5),
            "w3": _w((lambda *s: torch.randn(*s, dtype=torch.bfloat16) * 0.4)(D, F) * (2.0 / D) ** 0.5),
            "w2": _w((lambda *s: torch.randn(*s, dtype=torch.bfloat16) * 0.4)(F, D) * (2.0 / F) ** 0.5),
        }
    _B_in = _B_setup()

    return {
        "A_u": _A_in["u"],
        "A_A_re": _A_in["A_re"],
        "A_A_im": _A_in["A_im"],
        "A_B_re": _A_in["B_re"],
        "A_B_im": _A_in["B_im"],
        "A_C_re": _A_in["C_re"],
        "A_C_im": _A_in["C_im"],
        "A_log_dt": _A_in["log_dt"],
        "A_Dskip": _A_in["Dskip"],
        "B_x": _B_in["x"],
        "B_w1": _B_in["w1"],
        "B_w3": _B_in["w3"],
        "B_w2": _B_in["w2"],
    }

def reference(A_u, A_A_re, A_A_im, A_B_re, A_B_im, A_C_re, A_C_im, A_log_dt, A_Dskip, B_x, B_w1, B_w3, B_w2):
    def _A_ref(u, A_re, A_im, B_re, B_im, C_re, C_im, log_dt, Dskip):
        Bsz, L, D = u.shape
        dt = torch.exp(log_dt)[:, None]
        A = A_re + 1j * A_im
        Bc = B_re + 1j * B_im
        Cc = C_re + 1j * C_im
        logdA = dt * A
        dB = dt * Bc
        l = (lambda x, dt: x.to(dt))(torch.arange(L), A_re.dtype)
        P = torch.exp(logdA[:, :, None] * l[None, None, :])
        K = torch.einsum("dn,dnl,dn->dl", Cc, P, dB).real
        L2 = 2 * L
        Kp = _pad(K, ((0, 0), (0, L)))
        up = (lambda x, *d: x.permute(*d))(u, 0, 2, 1)
        up = _pad(up, ((0, 0), (0, 0), (0, L)))
        Kf = (lambda x, axes=(-2,-1): torch.fft.rfft2(x.float(), dim=axes))(Kp[:, None, :], axes=(-2, -1))
        Uf = (lambda x, axes=(-2,-1): torch.fft.rfft2(x.float(), dim=axes))(up[:, :, None, :], axes=(-2, -1))
        Yf = Uf * Kf[None, :, :, :]
        y = (lambda x, s, axes=(-2,-1): torch.fft.irfft2(x, s=s, dim=axes).to(torch.bfloat16))(Yf, (1, L2), axes=(-2, -1))[:, :, 0, :L]
        y = (lambda x, *d: x.permute(*d))(y, 0, 2, 1) + u * Dskip[None, None, :]
        return y

    def _B_ref(x, w1, w3, w2):
        gate = torch.einsum("bsd,df->bsf", x, w1)
        up = torch.einsum("bsd,df->bsf", x, w3)
        h = _silu(gate) * up
        out = torch.einsum("bsf,fd->bsd", h, w2)
        return out

    x = A_u
    h = x + _A_ref(_layernorm(x, axis=-1), A_A_re, A_A_im, A_B_re, A_B_im, A_C_re, A_C_im, A_log_dt, A_Dskip)
    h2 = h + _B_ref(_layernorm(h, axis=-1), B_w1, B_w3, B_w2)
    return h2
```

```cpp
#include <hip/hip_runtime.h>
#include <hip/hip_bf16.h>
#include <stdint.h>

typedef __bf16 bf16_t;
typedef __attribute__((ext_vector_type(2))) __bf16 bf16x2;
typedef __attribute__((ext_vector_type(4))) __bf16 bf16x4;
typedef __attribute__((ext_vector_type(8))) __bf16 bf16x8;
typedef __attribute__((ext_vector_type(2))) float f32x2;
typedef __attribute__((ext_vector_type(4))) float f32x4;
typedef __attribute__((ext_vector_type(16))) float f32x16;
typedef __attribute__((ext_vector_type(4))) unsigned u32x4;
typedef __attribute__((ext_vector_type(2))) unsigned u32x2;
#define LDSP __attribute__((address_space(3)))
#define GLBP __attribute__((address_space(1)))
#define DEV __device__ __forceinline__

constexpr int L = 2048, D = 576, NS = 128, F = 4608;
constexpr size_t SZ_LD = (size_t)L * D * 2;
constexpr size_t OFF_XT = 0, OFF_UR = SZ_LD, OFF_KG = 2 * SZ_LD, OFF_HT = 3 * SZ_LD, OFF_CS = 4 * SZ_LD;
constexpr size_t OFF_ST = OFF_CS + 16 * 9216 * 4;
constexpr size_t OFF_FL = OFF_ST + 2048 * 8;
constexpr size_t OFF_HM = (size_t)16 << 20;
constexpr size_t OFF_SL = (size_t)40 << 20;

DEV float bf_lo(unsigned v) { return __uint_as_float(v << 16); }
DEV float bf_hi(unsigned v) { return __uint_as_float(v & 0xffff0000u); }
DEV unsigned pack_bf(float a, float b) {
  bf16x2 r = {(__bf16)a, (__bf16)b};
  return __builtin_bit_cast(unsigned, r);
}
DEV float bf2f(bf16_t v) { return (float)v; }
DEV bf16x4 tr_read(const char LDSP* p) {
  return __builtin_amdgcn_ds_read_tr16_b64_v4bf16((bf16x4 LDSP*)p);
}
DEV bf16x8 cat8(bf16x4 a, bf16x4 b) { return __builtin_shufflevector(a, b, 0, 1, 2, 3, 4, 5, 6, 7); }

constexpr int NB_KG = D, NB_LN = L / 16, NB_CS = 18 * 16;
constexpr int LN_STRIDE = 1216;

DEV void cexp_rev(float ar, float ai, float pos, float& re, float& im) {
  float e = __builtin_amdgcn_exp2f(ar * pos);
  float th = __builtin_amdgcn_fractf(ai * pos);
  re = e * __builtin_amdgcn_cosf(th);
  im = e * __builtin_amdgcn_sinf(th);
}
DEV void split_pair(float a, float b, unsigned& hi, unsigned& lo) {
  hi = pack_bf(a, b);
  lo = pack_bf(a - bf_lo(hi), b - bf_hi(hi));
}

__global__ __launch_bounds__(256) void k_prep(const bf16_t* __restrict__ x, const bf16_t* __restrict__ Are,
                                              const bf16_t* __restrict__ Aim, const bf16_t* __restrict__ Bre,
                                              const bf16_t* __restrict__ Bim, const bf16_t* __restrict__ Cre,
                                              const bf16_t* __restrict__ Cim, const bf16_t* __restrict__ logdt,
                                              const bf16_t* __restrict__ w1, const bf16_t* __restrict__ w3,
                                              char* __restrict__ ws) {
  __shared__ __attribute__((aligned(16))) char smem[2 * 16 * LN_STRIDE];
  const int tid = threadIdx.x, lane = tid & 63, w = tid >> 6;
  int b = blockIdx.x;
  if (b < NB_KG) {
    const int d = b;
    f32x4* prm = (f32x4*)smem;
    f32x4* red = (f32x4*)(smem + 2048);
    if (tid < NS) {
      const int o = d * NS + tid;
      float dt = __expf(bf2f(logdt[d]));
      float are = bf2f(Are[o]), aim = bf2f(Aim[o]);
      float bre = dt * bf2f(Bre[o]), bim = dt * bf2f(Bim[o]);
      float cre = bf2f(Cre[o]), cim = bf2f(Cim[o]);
      float ar = (dt * are) * 1.44269504088896341f;
      ar = fminf(fmaxf(ar, -1e30f), 1e30f);
      float ai = (dt * aim) * 0.15915494309189535f;
      prm[tid] = f32x4{ar, ai, cre * bre - cim * bim, cre * bim + cim * bre};
    }
    __syncthreads();
    const int r = lane & 31, hk = lane >> 5;
    f32x16 acc0 = {}, acc1 = {};
    const float pf0 = (float)r, pf1 = (float)(32 + r), pc = (float)(64 * r);
#pragma unroll
    for (int ks = 0; ks < 4; ++ks) {
      u32x4 a0h, a0l, a1h, a1l, bh, bl;
#pragma unroll
      for (int e = 0; e < 4; ++e) {
        f32x4 p = prm[32 * w + 8 * ks + 4 * hk + e];
        float re, im;
        unsigned hi, lo;
        cexp_rev(p[0], p[1], pf0, re, im);
        split_pair(re, im, hi, lo); a0h[e] = hi; a0l[e] = lo;
        cexp_rev(p[0], p[1], pf1, re, im);
        split_pair(re, im, hi, lo); a1h[e] = hi; a1l[e] = lo;
        cexp_rev(p[0], p[1], pc, re, im);
        float gr = p[2] * re - p[3] * im, gi = p[2] * im + p[3] * re;
        split_pair(gr, -gi, hi, lo); bh[e] = hi; bl[e] = lo;
      }
      bf16x8 A0h = __builtin_bit_cast(bf16x8, a0h), A0l = __builtin_bit_cast(bf16x8, a0l);
      bf16x8 A1h = __builtin_bit_cast(bf16x8, a1h), A1l = __builtin_bit_cast(bf16x8, a1l);
      bf16x8 Bh = __builtin_bit_cast(bf16x8, bh), Bl = __builtin_bit_cast(bf16x8, bl);
      acc0 = __builtin_amdgcn_mfma_f32_32x32x16_bf16(A0l, Bh, acc0, 0, 0, 0);
      acc1 = __builtin_amdgcn_mfma_f32_32x32x16_bf16(A1l, Bh, acc1, 0, 0, 0);
      acc0 = __builtin_amdgcn_mfma_f32_32x32x16_bf16(A0h, Bl, acc0, 0, 0, 0);
      acc1 = __builtin_amdgcn_mfma_f32_32x32x16_bf16(A1h, Bl, acc1, 0, 0, 0);
      acc0 = __builtin_amdgcn_mfma_f32_32x32x16_bf16(A0h, Bh, acc0, 0, 0, 0);
      acc1 = __builtin_amdgcn_mfma_f32_32x32x16_bf16(A1h, Bh, acc1, 0, 0, 0);
    }
#pragma unroll
    for (int g = 0; g < 4; ++g) {
      red[(w * 8 + g) * 64 + lane] = f32x4{acc0[4 * g], acc0[4 * g + 1], acc0[4 * g + 2], acc0[4 * g + 3]};
      red[(w * 8 + 4 + g) * 64 + lane] = f32x4{acc1[4 * g], acc1[4 * g + 1], acc1[4 * g + 2], acc1[4 * g + 3]};
    }
    __syncthreads();
    bf16_t* Kg = (bf16_t*)(ws + OFF_KG) + (size_t)d * L;
#pragma unroll
    for (int it = 0; it < 2; ++it) {
      const int item = 2 * w + it;
      f32x4 s = red[item * 64 + lane] + red[(8 + item) * 64 + lane] + red[(16 + item) * 64 + lane] +
                red[(24 + item) * 64 + lane];
      const int l0 = 32 * (item >> 2) + 8 * (item & 3) + 4 * hk;
      u32x2 o = {pack_bf(s[0], s[1]), pack_bf(s[2], s[3])};
      *(u32x2*)(Kg + 64 * r + l0) = o;
    }
    return;
  }
  b -= NB_KG;
  if (b < NB_LN) {
    const int t0 = b * 16, rr = tid >> 4, j = tid & 15;
    if (b == 0) {
      u32x4* fl = (u32x4*)(ws + OFF_FL);
#pragma unroll
      for (int i = 0; i < 4; ++i) fl[tid * 4 + i] = u32x4{0, 0, 0, 0};
    }
    const u32x2* xr = (const u32x2*)(x + (size_t)(t0 + rr) * D);
    u32x2 v[9];
#pragma unroll
    for (int i = 0; i < 9; ++i) v[i] = xr[j + 16 * i];
    float s = 0.f;
#pragma unroll
    for (int i = 0; i < 9; ++i) s += (bf_lo(v[i][0]) + bf_hi(v[i][0])) + (bf_lo(v[i][1]) + bf_hi(v[i][1]));
#pragma unroll
    for (int m = 1; m < 16; m <<= 1) s += __shfl_xor(s, m, 64);
    const float mean = s * (1.f / D);
    float q = 0.f;
#pragma unroll
    for (int i = 0; i < 9; ++i) {
      float a = bf_lo(v[i][0]) - mean, bb = bf_hi(v[i][0]) - mean, c = bf_lo(v[i][1]) - mean, e = bf_hi(v[i][1]) - mean;
      q += (a * a + bb * bb) + (c * c + e * e);
    }
#pragma unroll
    for (int m = 1; m < 16; m <<= 1) q += __shfl_xor(q, m, 64);
    const float rstd = 1.f / sqrtf(q * (1.f / D) + 1e-5f);
    char* xs = smem + rr * LN_STRIDE;
    char* us = smem + 16 * LN_STRIDE + (15 - rr) * LN_STRIDE;
#pragma unroll
    for (int i = 0; i < 9; ++i) {
      *(u32x2*)(xs + 8 * (j + 16 * i)) = v[i];
      u32x2 o = {pack_bf((bf_lo(v[i][0]) - mean) * rstd, (bf_hi(v[i][0]) - mean) * rstd),
                 pack_bf((bf_lo(v[i][1]) - mean) * rstd, (bf_hi(v[i][1]) - mean) * rstd)};
      *(u32x2*)(us + 8 * (j + 16 * i)) = o;
    }
    __syncthreads();
    const int gq = lane >> 4, i16 = lane & 15, qq = i16 >> 2, pp = i16 & 3;
    for (int wt = w; wt < 18; wt += 4) {
      const int T = 4 * wt + gq, arr = T >= 36, dg = T - 36 * arr;
      const char LDSP* base = (const char LDSP*)smem + arr * 16 * LN_STRIDE + qq * LN_STRIDE + dg * 32 + pp * 8;
      bf16x4 r0 = tr_read(base), r1 = tr_read(base + 4 * LN_STRIDE), r2 = tr_read(base + 8 * LN_STRIDE),
             r3 = tr_read(base + 12 * LN_STRIDE);
      const int dd = 16 * dg + i16;
      bf16_t* dst = arr ? (bf16_t*)(ws + OFF_UR) + (size_t)dd * L + (2032 - t0)
                        : (bf16_t*)(ws + OFF_XT) + (size_t)dd * L + t0;
      *(bf16x8*)dst = cat8(r0, r1);
      *(bf16x8*)(dst + 8) = cat8(r2, r3);
    }
    return;
  }
  b -= NB_LN;
  {
    const int cblk = b % 18, rblk = b / 18;
    const bf16_t* W = cblk < 9 ? w1 : w3;
    const int c0 = (cblk % 9) * 512 + lane * 8;
    const int r0 = rblk * 36 + w * 9;
    u32x4 v[9];
#pragma unroll
    for (int i = 0; i < 9; ++i) v[i] = *(const u32x4*)(W + (size_t)(r0 + i) * F + c0);
    float s[8] = {};
#pragma unroll
    for (int i = 0; i < 9; ++i)
#pragma unroll
      for (int e = 0; e < 4; ++e) { s[2 * e] += bf_lo(v[i][e]); s[2 * e + 1] += bf_hi(v[i][e]); }
    f32x4* red = (f32x4*)smem;
    red[w * 128 + lane * 2] = f32x4{s[0], s[1], s[2], s[3]};
    red[w * 128 + lane * 2 + 1] = f32x4{s[4], s[5], s[6], s[7]};
    __syncthreads();
    if (tid < 128) {
      f32x4 t = red[tid] + red[128 + tid] + red[256 + tid] + red[384 + tid];
      *(f32x4*)((float*)(ws + OFF_CS) + rblk * 9216 + cblk * 512 + tid * 4) = t;
    }
  }
}

constexpr int CV_S = 4384;
constexpr int CV_K = 8 * CV_S;
constexpr int CV_LDS = CV_K + 4352;

__global__ __launch_bounds__(256) void k_conv(const bf16_t* __restrict__ dskip, char* __restrict__ ws) {
  __shared__ __attribute__((aligned(16))) char smem[CV_LDS];
  const int tid = threadIdx.x, lane = tid & 63, w = tid >> 6;
  const int d = blockIdx.x;
  const bf16_t* ur = (const bf16_t*)(ws + OFF_UR) + (size_t)d * L;
  const bf16_t* xt = (const bf16_t*)(ws + OFF_XT) + (size_t)d * L;
  const bf16_t* kg = (const bf16_t*)(ws + OFF_KG) + (size_t)d * L;
  u32x4 c0 = *(const u32x4*)(ur + tid * 8);
  u32x4 c1 = {0, 0, 0, 0};
  if (tid < 255) c1 = *(const u32x4*)(ur + tid * 8 + 8);
  u32x4 kk = *(const u32x4*)(kg + tid * 8);
  const int m16 = lane & 15, q4 = lane >> 4;
  const int tl = 128 * (q4 >> 1) + 8 * m16 + 4 * (q4 & 1);
  u32x2 xe[2], ue[2];
#pragma unroll
  for (int i = 0; i < 2; ++i) {
    const int t = 256 * (2 * w + i) + tl;
    xe[i] = *(const u32x2*)(xt + t);
    ue[i] = *(const u32x2*)(ur + (2044 - t));
  }
  const float dsk = bf2f(dskip[d]);
  {
    unsigned e[8] = {c0[0], c0[1], c0[2], c0[3], c1[0], c1[1], c1[2], c1[3]};
    unsigned o[7];
#pragma unroll
    for (int i = 0; i < 7; ++i) o[i] = __builtin_amdgcn_alignbyte(e[i + 1], e[i], 2);
#pragma unroll
    for (int s = 0; s < 8; ++s) {
      u32x4 v;
      if (s & 1) v = u32x4{o[s >> 1], o[(s >> 1) + 1], o[(s >> 1) + 2], o[(s >> 1) + 3]};
      else v = u32x4{e[s >> 1], e[(s >> 1) + 1], e[(s >> 1) + 2], e[(s >> 1) + 3]};
      *(u32x4*)(smem + (7 - s) * CV_S + tid * 16) = v;
    }
    if (tid < 128) *(u32x4*)(smem + (tid >> 4) * CV_S + 4096 + (tid & 15) * 16) = u32x4{0, 0, 0, 0};
    *(u32x4*)(smem + CV_K + 256 + tid * 16) = kk;
    if (tid < 16) *(u32x4*)(smem + CV_K + tid * 16) = u32x4{0, 0, 0, 0};
  }
  __syncthreads();
  f32x4 acc[8] = {};
  {
    const int phi = m16 & 7, nn = m16 >> 3;
    const char* ab = smem + phi * CV_S + 2 * (1920 + 8 * q4 - 128 * nn);
    const char* bb = smem + CV_K + 2 * (8 * m16 + 8 + 8 * q4);
#pragma unroll
    for (int k2 = 0; k2 < 2; ++k2) {
      const int ks = 2 * w + k2;
      bf16x8 A[8], B[8];
#pragma unroll
      for (int g = 0; g < 8; ++g) {
        A[g] = *(const bf16x8*)(ab + 64 * ks - 512 * g);
        B[g] = *(const bf16x8*)(bb + 64 * ks + 512 * g);
      }
#pragma unroll
      for (int g = 0; g < 8; ++g)
#pragma unroll
        for (int i = 0; i + g < 8; ++i)
          acc[g + i] = __builtin_amdgcn_mfma_f32_16x16x32_bf16(A[g], B[i], acc[g + i], 0, 0, 0);
    }
  }
  __syncthreads();
  f32x4* red = (f32x4*)smem;
#pragma unroll
  for (int g = 0; g < 8; ++g)
    if ((g >> 1) != w) red[(w * 8 + g) * 64 + lane] = acc[g];
  __syncthreads();
  bf16_t* ht = (bf16_t*)(ws + OFF_HT) + (size_t)d * L;
#pragma unroll
  for (int i = 0; i < 2; ++i) {
    f32x4 s = {};
#pragma unroll
    for (int g = 0; g < 8; ++g)
      if (g == 2 * w + i) s = acc[g];
#pragma unroll
    for (int ww = 0; ww < 4; ++ww)
      if (ww != w) s += red[(ww * 8 + 2 * w + i) * 64 + lane];
    const int t = 256 * (2 * w + i) + tl;
    float h0 = bf_lo(xe[i][0]) + (s[0] + bf_hi(ue[i][1]) * dsk);
    float h1 = bf_hi(xe[i][0]) + (s[1] + bf_lo(ue[i][1]) * dsk);
    float h2 = bf_lo(xe[i][1]) + (s[2] + bf_hi(ue[i][0]) * dsk);
    float h3 = bf_hi(xe[i][1]) + (s[3] + bf_lo(ue[i][0]) * dsk);
    *(u32x2*)(ht + t) = u32x2{pack_bf(h0, h1), pack_bf(h2, h3)};
  }
}

__global__ __launch_bounds__(256) void k_stats(char* __restrict__ ws) {
  __shared__ float red[2][128][17];
  const int tid = threadIdx.x, tq = tid & 1, ds = tid >> 1;
  const int t0 = blockIdx.x * 16 + tq * 8;
  const bf16_t* ht = (const bf16_t*)(ws + OFF_HT);
  float s[8] = {}, q[8] = {};
  for (int d = ds; d < D; d += 128) {
    u32x4 v = *(const u32x4*)(ht + (size_t)d * L + t0);
#pragma unroll
    for (int e = 0; e < 4; ++e) {
      float a = bf_lo(v[e]), b = bf_hi(v[e]);
      s[2 * e] += a; q[2 * e] += a * a; s[2 * e + 1] += b; q[2 * e + 1] += b * b;
    }
  }
#pragma unroll
  for (int e = 0; e < 8; ++e) { red[0][ds][tq * 8 + e] = s[e]; red[1][ds][tq * 8 + e] = q[e]; }
  __syncthreads();
  if (tid < 16) {
    float S = 0, Q = 0;
    for (int i = 0; i < 128; ++i) { S += red[0][i][tid]; Q += red[1][i][tid]; }
    float mean = S * (1.f / D);
    float var = fmaxf(Q * (1.f / D) - mean * mean, 0.f);
    ((f32x2*)(ws + OFF_ST))[blockIdx.x * 16 + tid] = f32x2{mean, 1.f / sqrtf(var + 1e-5f)};
  }
}

#define WAIT_V(n) asm volatile("s_waitcnt vmcnt(%0)" ::"n"(n) : "memory")
#define BAR() do { __builtin_amdgcn_s_barrier(); asm volatile("" ::: "memory"); } while (0)
#define SCHED() __builtin_amdgcn_sched_barrier(0)
constexpr int G1_HB = 32 * 512, G1_WB = 32 * 576, G1_STAGE = G1_HB + G1_WB;
constexpr int G1_NST = 4, G1_CSOFF = G1_NST * G1_STAGE, G1_LDS = G1_CSOFF + 288 * 4;

DEV void glds16(const void* g, unsigned lds) {
  unsigned keep;
  asm volatile("s_mov_b32 %0, m0\n\ts_mov_b32 m0, %2\n\ts_nop 0\n\tglobal_load_lds_dwordx4 %1, off\n\ts_mov_b32 m0, %0"
               : "=&s"(keep) : "v"(g), "s"(lds) : "memory");
}
DEV unsigned lds_addr(const void* p) { return (unsigned)(size_t)(const char LDSP*)p; }

template <int NT, int N0>
DEV void gemm1_body(char* smem, char* ws, const char* const (&hsrc)[2], const char* const (&wsrc)[3], int w, int lane,
                    int t0, int f0) {
  const int tw = w & 3;
  auto stage = [&](int s) {
    const unsigned sb = lds_addr(smem) + (s % G1_NST) * G1_STAGE;
#pragma unroll
    for (int i = 0; i < 2; ++i) glds16(hsrc[i] + (size_t)s * (32 * L * 2), sb + (2 * w + i) * 1024);
#pragma unroll
    for (int i = 0; i < 2; ++i) glds16(wsrc[i] + (size_t)s * (32 * F * 2), sb + G1_HB + w * 2304 + i * 1024);
    if (lane < 16) glds16(wsrc[2] + (size_t)s * (32 * F * 2), sb + G1_HB + w * 2304 + 2048);
  };
  const int gq = lane >> 4, i16 = lane & 15, qq = i16 >> 2, pp = i16 & 3, hk = gq >> 1, g1 = gq & 1;
  const int wlane = G1_HB + (8 * hk + qq) * 576 + g1 * 288 + (((pp & 1) << 1) | (pp >> 1)) * 8 + N0 * 32;
  const int hlane0 = (8 * hk + qq) * 512 + (((2 * tw) ^ qq) * 64) + g1 * 32 + pp * 8;
  const int hlane1 = (8 * hk + qq) * 512 + (((2 * tw + 1) ^ qq) * 64) + g1 * 32 + pp * 8;

  f32x16 acc[NT][2] = {};
  bf16x8 Wf[2][NT], Hf[2][2];
  auto load_frags = [&](int buf, int s, int ksub) {
    const char LDSP* sb = (const char LDSP*)smem + (s % G1_NST) * G1_STAGE;
    Hf[buf][0] = cat8(tr_read(sb + hlane0 + (16 * ksub) * 512), tr_read(sb + hlane0 + (16 * ksub + 4) * 512));
    Hf[buf][1] = cat8(tr_read(sb + hlane1 + (16 * ksub) * 512), tr_read(sb + hlane1 + (16 * ksub + 4) * 512));
#pragma unroll
    for (int n = 0; n < NT; ++n)
      Wf[buf][n] = cat8(tr_read(sb + wlane + (16 * ksub) * 576 + n * 32),
                        tr_read(sb + wlane + (16 * ksub + 4) * 576 + n * 32));
  };
  auto mma = [&](int buf) {
#pragma unroll
    for (int n = 0; n < NT; ++n) {
      acc[n][0] = __builtin_amdgcn_mfma_f32_32x32x16_bf16(Wf[buf][n], Hf[buf][0], acc[n][0], 0, 0, 0);
      acc[n][1] = __builtin_amdgcn_mfma_f32_32x32x16_bf16(Wf[buf][n], Hf[buf][1], acc[n][1], 0, 0, 0);
    }
  };
  constexpr int NST = D / 32;
  WAIT_V(10);
  BAR();
  load_frags(0, 0, 0);
#pragma unroll
  for (int s = 0; s < NST; ++s) {
    if (s + 1 < NST) {
      if (s + 2 < NST) WAIT_V(5); else WAIT_V(0);
      BAR();
    }
    if (s + 3 < NST) stage(s + 3);
    load_frags(1, s, 1);
    mma(0);
    if (s + 1 < NST) load_frags(0, s + 1, 0);
    mma(1);
    SCHED();
  }

  const float* csl = (const float*)(smem + G1_CSOFF);
  const int c31 = lane & 31, h = lane >> 5;
  char* hm = ws + OFF_HM;
#pragma unroll
  for (int mt = 0; mt < 2; ++mt) {
    const int t = t0 + 64 * tw + 32 * mt + c31;
    const f32x2 st = ((const f32x2*)(ws + OFF_ST))[t];
    const float nmu = -st[0], sc = -1.44269504088896341f * st[1], ir2 = 1.f / (st[1] * st[1]);
#pragma unroll
    for (int n = 0; n < NT; ++n) {
      const int fl = 16 * (N0 + n) + 8 * h;
      f32x4 c1a = *(const f32x4*)(csl + fl), c1b = *(const f32x4*)(csl + fl + 4);
      f32x4 c3a = *(const f32x4*)(csl + 144 + fl), c3b = *(const f32x4*)(csl + 144 + fl + 4);
      float o[8];
#pragma unroll
      for (int e = 0; e < 8; ++e) {
        float g = acc[n][mt][e] + nmu * (e < 4 ? c1a[e & 3] : c1b[e & 3]);
        float u = acc[n][mt][8 + e] + nmu * (e < 4 ? c3a[e & 3] : c3b[e & 3]);
        float ex = __builtin_amdgcn_exp2f(g * sc);
        float den = ex * ir2 + ir2;
        o[e] = (g * u) * __builtin_amdgcn_rcpf(den);
      }
      const int f = f0 + fl;
      const size_t off = ((size_t)((t >> 4) * 144 + (f >> 5)) * 64 + (t & 15) + 16 * ((f >> 3) & 3)) * 16;
      *(u32x4*)(hm + off) = u32x4{pack_bf(o[0], o[1]), pack_bf(o[2], o[3]), pack_bf(o[4], o[5]), pack_bf(o[6], o[7])};
    }
  }
}

__global__ __launch_bounds__(512, 2) void k_gemm1(const bf16_t* __restrict__ w1, const bf16_t* __restrict__ w3,
                                                  char* __restrict__ ws) {
  extern __shared__ __attribute__((aligned(16))) char smem[];
  const int tid = threadIdx.x, lane = tid & 63;
  const int w = __builtin_amdgcn_readfirstlane(tid >> 6);
  const int bid = blockIdx.x, xg = bid & 7, loc = bid >> 3;
  const int mt_ = 4 * (xg & 1) + (loc & 3), nt_ = 8 * (xg >> 1) + (loc >> 2);
  const int t0 = mt_ * 256, f0 = nt_ * 144;

  if (tid < 288) {
    const float* cs = (const float*)(ws + OFF_CS);
    const int col = tid < 144 ? f0 + tid : F + f0 + (tid - 144);
    float s = 0.f;
#pragma unroll
    for (int i = 0; i < 16; ++i) s += cs[i * 9216 + col];
    ((float*)(smem + G1_CSOFF))[tid] = s;
  }

  const char* hsrc[2];
  const char* wsrc[3];
  {
    const char* hT = ws + OFF_HT;
#pragma unroll
    for (int i = 0; i < 2; ++i) {
      const int k = 2 * (2 * w + i) + (lane >> 5), c = ((lane & 31) >> 2) ^ (k & 3);
      hsrc[i] = hT + (size_t)k * (L * 2) + t0 * 2 + c * 64 + (lane & 3) * 16;
    }
#pragma unroll
    for (int i = 0; i < 3; ++i) {
      int o = i * 1024 + lane * 16;
      if (o >= 2304) o -= 768;
      const int row = 4 * w + o / 576, c = o % 576;
      const char* base = (const char*)(c < 288 ? w1 : w3);
      wsrc[i] = base + (size_t)row * (F * 2) + f0 * 2 + (c < 288 ? c : c - 288);
    }
  }
#pragma unroll
  for (int s = 0; s < 3; ++s) {
    const unsigned sb = lds_addr(smem) + s * G1_STAGE;
#pragma unroll
    for (int i = 0; i < 2; ++i) glds16(hsrc[i] + (size_t)s * (32 * L * 2), sb + (2 * w + i) * 1024);
#pragma unroll
    for (int i = 0; i < 2; ++i) glds16(wsrc[i] + (size_t)s * (32 * F * 2), sb + G1_HB + w * 2304 + i * 1024);
    if (lane < 16) glds16(wsrc[2] + (size_t)s * (32 * F * 2), sb + G1_HB + w * 2304 + 2048);
  }
  if (w < 4) gemm1_body<5, 0>(smem, ws, hsrc, wsrc, w, lane, t0, f0);
  else gemm1_body<4, 5>(smem, ws, hsrc, wsrc, w, lane, t0, f0);
}

constexpr int G2_STAGE = 64 * 288;
constexpr int G2_NST = 5, G2_LDS = G2_NST * G2_STAGE;

__global__ __launch_bounds__(256, 1) void k_gemm2(const bf16_t* __restrict__ w2, char* __restrict__ ws,
                                                  bf16_t* __restrict__ out) {
  extern __shared__ __attribute__((aligned(16))) char smem[];
  const int tid = threadIdx.x, lane = tid & 63;
  const int w = __builtin_amdgcn_readfirstlane(tid >> 6);
  const int bid = blockIdx.x, xg = bid & 7, loc = bid >> 3;
  const int ksp = xg >> 1, mt_ = 8 * (xg & 1) + (loc & 7), nt_ = loc >> 3;
  const int t0 = mt_ * 128, d0 = nt_ * 144, k0 = ksp * 1152;
  const int tile = mt_ * 4 + nt_;

  const char* wsrc[5];
#pragma unroll
  for (int i = 0; i < 5; ++i) {
    int o = i * 1024 + lane * 16;
    if (o >= 4608) o -= 512;
    const int rho = 16 * w + o / 288, c = o % 288;
    const int k = (rho & ~12) | ((rho & 4) << 1) | ((rho & 8) >> 1);
    wsrc[i] = (const char*)w2 + (size_t)(k0 + k) * (D * 2) + d0 * 2 + c;
  }
  const char* hsrc = ws + OFF_HM + ((size_t)((t0 + 32 * w) >> 4) * 144 + (k0 >> 5)) * 1024 + lane * 16;

  bf16x8 Hf[4][2][2];
  auto issue = [&](int s) {
    const unsigned sb = lds_addr(smem) + (s % G2_NST) * G2_STAGE;
#pragma unroll
    for (int i = 0; i < 4; ++i)
      glds16(wsrc[i] + (size_t)s * (64 * D * 2), sb + w * 4608 + i * 1024);
    if (lane < 32) glds16(wsrc[4] + (size_t)s * (64 * D * 2), sb + w * 4608 + 4096);
#pragma unroll
    for (int ks = 0; ks < 2; ++ks)
#pragma unroll
      for (int tt = 0; tt < 2; ++tt) {
        const char* p = hsrc + (size_t)tt * (144 * 1024) + (2 * s + ks) * 1024;
        asm volatile("global_load_dwordx4 %0, %1, off" : "=v"(Hf[s & 3][ks][tt]) : "v"(p) : "memory");
      }
  };
  issue(0); issue(1); issue(2);

  const int kq = lane >> 4, i16 = lane & 15, qq = i16 >> 2, pp = i16 & 3;
  const int wlane = (16 * (kq >> 1) + 4 * (kq & 1) + qq) * 288 + pp * 8;

  f32x4 acc[9][2] = {};
  bf16x8 Wf[2][9];
  auto load_frags = [&](int buf, int s, int ksub) {
    const char LDSP* sb = (const char LDSP*)smem + (s % G2_NST) * G2_STAGE + wlane + ksub * 32 * 288;
#pragma unroll
    for (int n = 0; n < 9; ++n) Wf[buf][n] = cat8(tr_read(sb + n * 32), tr_read(sb + 8 * 288 + n * 32));
  };
  auto mma = [&](int buf, int s, int ksub) {
#pragma unroll
    for (int n = 0; n < 9; ++n) {
      acc[n][0] = __builtin_amdgcn_mfma_f32_16x16x32_bf16(Wf[buf][n], Hf[s & 3][ksub][0], acc[n][0], 0, 0, 0);
      acc[n][1] = __builtin_amdgcn_mfma_f32_16x16x32_bf16(Wf[buf][n], Hf[s & 3][ksub][1], acc[n][1], 0, 0, 0);
    }
  };
#define PIN_H(s)                                                                                       \
  asm volatile("" : "+v"(Hf[(s) & 3][0][0]), "+v"(Hf[(s) & 3][0][1]), "+v"(Hf[(s) & 3][1][0]), \
               "+v"(Hf[(s) & 3][1][1]))
  constexpr int NST = 18;
  WAIT_V(18);
  PIN_H(0);
  BAR();
  load_frags(0, 0, 0);
#pragma unroll
  for (int s = 0; s < NST; ++s) {
    if (s + 1 < NST) {
      if (s + 2 < NST) WAIT_V(9); else WAIT_V(0);
      PIN_H(s + 1);
      BAR();
    }
    if (s + 3 < NST) issue(s + 3);
    load_frags(1, s, 1);
    mma(0, s, 0);
    if (s + 1 < NST) load_frags(0, s + 1, 0);
    mma(1, s, 1);
    SCHED();
  }

  auto rs = __builtin_amdgcn_make_buffer_rsrc(ws + OFF_SL, 0, 64 * 4 * 4 * 18 * 1024, 0x00020000);
  const int lo = ksp < 2 ? 5 * ksp : 10 + 4 * (ksp - 2), cnt = ksp < 2 ? 5 : 4;
  const unsigned slab_w = (unsigned)(((tile * 4 + ksp) * 4 + w) * 18) * 1024u + lane * 16;
#pragma unroll
  for (int it = 0; it < 18; ++it) {
    if (it < lo || it >= lo + cnt)
      __builtin_amdgcn_raw_buffer_store_b128(__builtin_bit_cast(u32x4, acc[it >> 1][it & 1]), rs, slab_w + it * 1024, 0, 16);
  }
  asm volatile("s_waitcnt vmcnt(0)" ::: "memory");
  unsigned* flags = (unsigned*)(ws + OFF_FL) + (tile * 4 + w) * 16;
  if (lane == 0) __hip_atomic_store(flags + ksp, 1u, __ATOMIC_RELAXED, __HIP_MEMORY_SCOPE_AGENT);
  const bf16_t* hT = (const bf16_t*)(ws + OFF_HT);
  float hres[5][4];
#pragma unroll
  for (int j = 0; j < 5; ++j) {
    const int it = lo + (j < cnt ? j : 0);
    const int t = t0 + 32 * w + 16 * (it & 1) + i16, dd = d0 + 16 * (it >> 1) + 4 * kq;
#pragma unroll
    for (int e = 0; e < 4; ++e) hres[j][e] = bf2f(hT[(size_t)(dd + e) * L + t]);
  }
  {
    unsigned spins = 0;
    while (true) {
      unsigned v = 1;
      if (lane < 4) v = __hip_atomic_load(flags + lane, __ATOMIC_RELAXED, __HIP_MEMORY_SCOPE_AGENT);
      if (__all(v != 0)) break;
      if (++spins > (1u << 22)) break;
      __builtin_amdgcn_s_sleep(1);
    }
  }
  __builtin_amdgcn_fence(__ATOMIC_ACQUIRE, "wavefront");
  f32x4 part[5][3];
#pragma unroll
  for (int j = 0; j < 5; ++j)
#pragma unroll
    for (int o = 0; o < 3; ++o) {
      const int src = o + (o >= ksp);
      const int it = lo + (j < cnt ? j : 0);
      const unsigned off = (unsigned)(((tile * 4 + src) * 4 + w) * 18 + it) * 1024u + lane * 16;
      part[j][o] = __builtin_bit_cast(f32x4, __builtin_amdgcn_raw_buffer_load_b128(rs, off, 0, 16));
    }
#pragma unroll
  for (int j = 0; j < 5; ++j) {
    if (j < cnt) {
      const int it = lo + j;
      f32x4 own = {};
#pragma unroll
      for (int i2 = 0; i2 < 18; ++i2)
        if (i2 == it) own = acc[i2 >> 1][i2 & 1];
      f32x4 p0 = ksp == 0 ? own : part[j][0];
      f32x4 p1 = ksp == 1 ? own : (ksp < 1 ? part[j][0] : part[j][1]);
      f32x4 p2 = ksp == 2 ? own : (ksp < 2 ? part[j][1] : part[j][2]);
      f32x4 p3 = ksp == 3 ? own : part[j][2];
      f32x4 s = (p0 + p1) + (p2 + p3);
      const int t = t0 + 32 * w + 16 * (it & 1) + i16, dd = d0 + 16 * (it >> 1) + 4 * kq;
      u32x2 o = {pack_bf(hres[j][0] + s[0], hres[j][1] + s[1]), pack_bf(hres[j][2] + s[2], hres[j][3] + s[3])};
      *(u32x2*)(out + (size_t)t * D + dd) = o;
    }
  }
}

extern "C" void kernel_launch(void* const* d_in, const int* in_sizes, int n_in, void* d_out, int out_size,
                              void* d_ws, size_t ws_size, hipStream_t stream) {
  const bf16_t* x = (const bf16_t*)d_in[0];
  const bf16_t* Are = (const bf16_t*)d_in[1];
  const bf16_t* Aim = (const bf16_t*)d_in[2];
  const bf16_t* Bre = (const bf16_t*)d_in[3];
  const bf16_t* Bim = (const bf16_t*)d_in[4];
  const bf16_t* Cre = (const bf16_t*)d_in[5];
  const bf16_t* Cim = (const bf16_t*)d_in[6];
  const bf16_t* logdt = (const bf16_t*)d_in[7];
  const bf16_t* dskip = (const bf16_t*)d_in[8];
  const bf16_t* w1 = (const bf16_t*)d_in[10];
  const bf16_t* w3 = (const bf16_t*)d_in[11];
  const bf16_t* w2 = (const bf16_t*)d_in[12];
  char* ws = (char*)d_ws;
  (void)hipFuncSetAttribute((const void*)k_gemm1, hipFuncAttributeMaxDynamicSharedMemorySize, G1_LDS);
  (void)hipFuncSetAttribute((const void*)k_gemm2, hipFuncAttributeMaxDynamicSharedMemorySize, G2_LDS);
  k_prep<<<NB_KG + NB_LN + NB_CS, 256, 0, stream>>>(x, Are, Aim, Bre, Bim, Cre, Cim, logdt, w1, w3, ws);
  k_conv<<<D, 256, 0, stream>>>(dskip, ws);
  k_stats<<<L / 16, 256, 0, stream>>>(ws);
  k_gemm1<<<256, 512, G1_LDS, stream>>>(w1, w3, ws);
  k_gemm2<<<256, 256, G2_LDS, stream>>>(w2, ws, (bf16_t*)d_out);
}
```

```cpp
#include <hip/hip_runtime.h>
#include <hip/hip_bf16.h>
#include <stdint.h>

typedef __bf16 bf16_t;
typedef __attribute__((ext_vector_type(2))) __bf16 bf16x2;
typedef __attribute__((ext_vector_type(4))) __bf16 bf16x4;
typedef __attribute__((ext_vector_type(8))) __bf16 bf16x8;
typedef __attribute__((ext_vector_type(2))) float f32x2;
typedef __attribute__((ext_vector_type(4))) float f32x4;
typedef __attribute__((ext_vector_type(16))) float f32x16;
typedef __attribute__((ext_vector_type(4))) unsigned u32x4;
typedef __attribute__((ext_vector_type(2))) unsigned u32x2;
#define LDSP __attribute__((address_space(3)))
#define GLBP __attribute__((address_space(1)))
#define DEV __device__ __forceinline__

constexpr int L = 2048, D = 576, NS = 128, F = 4608;
constexpr size_t SZ_LD = (size_t)L * D * 2;
constexpr size_t OFF_XT = 0, OFF_UR = SZ_LD, OFF_KG = 2 * SZ_LD, OFF_HT = 3 * SZ_LD, OFF_CS = 4 * SZ_LD;
constexpr size_t OFF_ST = OFF_CS + 16 * 9216 * 4;
constexpr size_t OFF_FL = OFF_ST + 2048 * 8;
constexpr size_t OFF_CF = OFF_FL + 16384;
constexpr size_t OFF_HM = (size_t)16 << 20;
constexpr size_t OFF_SL = (size_t)40 << 20;

#define STAMP(kid)
#define MARK(i)
#define STAMP_ARG
#define STAMP_PASS
DEV float bf_lo(unsigned v) { return __uint_as_float(v << 16); }
DEV float bf_hi(unsigned v) { return __uint_as_float(v & 0xffff0000u); }
DEV unsigned pack_bf(float a, float b) {
  bf16x2 r = {(__bf16)a, (__bf16)b};
  return __builtin_bit_cast(unsigned, r);
}
#define STFLAV "sc1"
#define STASM 1
DEV void st16(void* p, u32x4 v) {
  asm volatile("global_store_dwordx4 %0, %1, off " STFLAV "\n\ts_nop 1" ::"v"(p), "v"(v) : "memory");
}
DEV void st8(void* p, u32x2 v) {
  asm volatile("global_store_dwordx2 %0, %1, off " STFLAV "\n\ts_nop 1" ::"v"(p), "v"(v) : "memory");
}
DEV float bf2f(bf16_t v) { return (float)v; }
DEV bf16x4 tr_read(const char LDSP* p) {
  return __builtin_amdgcn_ds_read_tr16_b64_v4bf16((bf16x4 LDSP*)p);
}
DEV bf16x8 cat8(bf16x4 a, bf16x4 b) { return __builtin_shufflevector(a, b, 0, 1, 2, 3, 4, 5, 6, 7); }

constexpr int NB_KG = D, NB_LN = 256;
typedef __attribute__((ext_vector_type(2))) _Float16 f16x2;
typedef __attribute__((ext_vector_type(8))) _Float16 f16x8;

DEV void cexp_rev(float ar, float ai, float pos, float& re, float& im) {
  float e = __builtin_amdgcn_exp2f(ar * pos);
  float th = __builtin_amdgcn_fractf(ai * pos);
  re = e * __builtin_amdgcn_cosf(th);
  im = e * __builtin_amdgcn_sinf(th);
}
DEV unsigned pack_h(float a, float b) {
  f16x2 r = {(_Float16)a, (_Float16)b};
  return __builtin_bit_cast(unsigned, r);
}
template <int CTRL>
DEV float dpp(float x) {
  return __builtin_bit_cast(float, __builtin_amdgcn_update_dpp(0, __builtin_bit_cast(int, x), CTRL, 0xf, 0xf, true));
}
DEV float wave_max(float x) {
  x = fmaxf(x, dpp<0x128>(x));
  x = fmaxf(x, dpp<0x124>(x));
  x = fmaxf(x, dpp<0x122>(x));
  x = fmaxf(x, dpp<0x121>(x));
  auto a = __builtin_amdgcn_permlane16_swap(__float_as_uint(x), __float_as_uint(x), false, false);
  x = fmaxf(__uint_as_float(a[0]), __uint_as_float(a[1]));
  auto b = __builtin_amdgcn_permlane32_swap(__float_as_uint(x), __float_as_uint(x), false, false);
  return fmaxf(__uint_as_float(b[0]), __uint_as_float(b[1]));
}
DEV float dot2bf(unsigned a, unsigned b, float c) {
  return __builtin_amdgcn_fdot2_f32_bf16(__builtin_bit_cast(bf16x2, a), __builtin_bit_cast(bf16x2, b), c, false);
}

__global__ __launch_bounds__(256) void k_prep(const bf16_t* __restrict__ x, const bf16_t* __restrict__ Are,
                                              const bf16_t* __restrict__ Aim, const bf16_t* __restrict__ Bre,
                                              const bf16_t* __restrict__ Bim, const bf16_t* __restrict__ Cre,
                                              const bf16_t* __restrict__ Cim, const bf16_t* __restrict__ logdt,
                                              char* __restrict__ ws) {
  __shared__ __attribute__((aligned(16))) char smem[4096 + 32768];
  STAMP(0);
  const int tid = threadIdx.x, lane = tid & 63, w = tid >> 6;
  int b = blockIdx.x;
  if (b < NB_KG) {
    const int d = b;
    f32x4* prm = (f32x4*)smem + w * 64;
    f32x4* red = (f32x4*)(smem + 4096);
    const int o = d * (NS / 2) + lane;
    const unsigned vare = ((const unsigned*)Are)[o];
    unsigned vaim = ((const unsigned*)Aim)[o];
    const unsigned vbre = ((const unsigned*)Bre)[o], vbim = ((const unsigned*)Bim)[o];
    const unsigned vcre = ((const unsigned*)Cre)[o], vcim = ((const unsigned*)Cim)[o];
    float dt = bf2f(logdt[d]);
    asm volatile("" : "+v"(vaim), "+v"(dt));
    dt = __expf(dt);
    MARK(0);
    asm volatile("s_waitcnt vmcnt(0)" ::: "memory");
    MARK(1);
    float ar[2], ai[2], cr[2], ci[2];
#pragma unroll
    for (int jj = 0; jj < 2; ++jj) {
      const float are = jj ? bf_hi(vare) : bf_lo(vare), aim = jj ? bf_hi(vaim) : bf_lo(vaim);
      const float bre = dt * (jj ? bf_hi(vbre) : bf_lo(vbre)), bim = dt * (jj ? bf_hi(vbim) : bf_lo(vbim));
      const float cre = jj ? bf_hi(vcre) : bf_lo(vcre), cim = jj ? bf_hi(vcim) : bf_lo(vcim);
      ar[jj] = __builtin_amdgcn_fmed3f((dt * are) * 1.44269504088896341f, -1e30f, 1e30f);
      ai[jj] = (dt * aim) * 0.15915494309189535f;
      cr[jj] = cre * bre - cim * bim;
      ci[jj] = cre * bim + cim * bre;
    }
    const float armax = wave_max(fmaxf(fmaxf(ar[0], ar[1]), 0.f));
    const float S = wave_max(fmaxf(fmaxf(fabsf(cr[0]), fabsf(ci[0])), fmaxf(fabsf(cr[1]), fabsf(ci[1]))));
    const float inv = S > 0.f ? 0.5f * __builtin_amdgcn_rcpf(S) : 0.f;
    if ((lane >> 4) == w) {
      prm[2 * (lane & 15)] = f32x4{ar[0] - armax, ai[0], cr[0] * inv, ci[0] * inv};
      prm[2 * (lane & 15) + 1] = f32x4{ar[1] - armax, ai[1], cr[1] * inv, ci[1] * inv};
    }
    __builtin_amdgcn_wave_barrier();
    {
      const f32x4 p = prm[lane & 31];
      float re, im;
      cexp_rev(p[0], p[1], lane < 32 ? 4.f : 1024.f, re, im);
      ((f32x2*)(prm + 32))[2 * (lane & 31) + (lane >> 5)] = f32x2{re, im};
    }
    __builtin_amdgcn_wave_barrier();
    MARK(2);
    const int r = lane & 15, kq = lane >> 4;
    const float pf = (float)(16 * (r >> 2) + (r & 3)), pc = (float)(64 * r);
    f32x4 acc[4][2] = {};
#pragma unroll
    for (int ks = 0; ks < 2; ++ks) {
      u32x4 A[4], B[2];
#pragma unroll
      for (int e = 0; e < 4; ++e) {
        const f32x4 p = prm[16 * ks + 4 * kq + e], c = prm[32 + 16 * ks + 4 * kq + e];
        float re, im;
        cexp_rev(p[0], p[1], pf, re, im);
        A[0][e] = pack_h(re, im);
#pragma unroll
        for (int ft = 1; ft < 4; ++ft) {
          const float nr = re * c[0] - im * c[1], ni = re * c[1] + im * c[0];
          re = nr; im = ni;
          A[ft][e] = pack_h(re, im);
        }
        cexp_rev(p[0], p[1], pc, re, im);
        const float gr = p[2] * re - p[3] * im, gi = p[2] * im + p[3] * re;
        B[0][e] = pack_h(gr, -gi);
        B[1][e] = pack_h(gr * c[2] - gi * c[3], -(gr * c[3] + gi * c[2]));
      }
#pragma unroll
      for (int ft = 0; ft < 4; ++ft)
#pragma unroll
        for (int ct = 0; ct < 2; ++ct)
          acc[ft][ct] = __builtin_amdgcn_mfma_f32_16x16x32_f16(__builtin_bit_cast(f16x8, A[ft]),
                                                               __builtin_bit_cast(f16x8, B[ct]), acc[ft][ct], 0, 0, 0);
    }
#pragma unroll
    for (int ft = 0; ft < 4; ++ft)
#pragma unroll
      for (int ct = 0; ct < 2; ++ct) red[(w * 8 + ct * 4 + ft) * 64 + lane] = acc[ft][ct];
    MARK(3);
    __syncthreads();
    MARK(4);
    f32x4 sv[2];
#pragma unroll
    for (int it = 0; it < 2; ++it) {
      const int item = 2 * w + it;
      sv[it] = (red[item * 64 + lane] + red[(8 + item) * 64 + lane]) +
               (red[(16 + item) * 64 + lane] + red[(24 + item) * 64 + lane]);
    }
    const int l = 64 * (16 * (w >> 1) + r) + 16 * kq + 8 * (w & 1);
    const float S2 = inv > 0.f ? 1.f / inv : 0.f;
    float ov[8];
#pragma unroll
    for (int e = 0; e < 8; ++e) ov[e] = sv[e >> 2][e & 3] * S2;
    if (armax > 0.f) {
#pragma unroll
      for (int e = 0; e < 8; ++e) ov[e] *= __builtin_amdgcn_exp2f(armax * (float)(l + e));
    }
    st16((bf16_t*)(ws + OFF_KG) + (size_t)d * L + l,
        u32x4{pack_bf(ov[0], ov[1]), pack_bf(ov[2], ov[3]), pack_bf(ov[4], ov[5]), pack_bf(ov[6], ov[7])});
    return;
  }
  b -= NB_KG;
  {
    const int rt = (b & 7) * 8 + (b >> 5), cs = (b >> 3) & 3;
    const int t0 = rt * 32, row = tid >> 3, j = tid & 7;
    const u32x4* xr = (const u32x4*)(x + (size_t)(t0 + row) * D);
    u32x4 v[9];
#pragma unroll
    for (int i = 0; i < 9; ++i) v[i] = xr[j + 8 * i];
    const u32x4 s0 = xr[18 * cs + j], s1 = xr[18 * cs + 8 + j], s2 = xr[18 * cs + 16 + (j & 1)];
    if (cs == 0) {
      if (tid < 32) ((u32x2*)(ws + OFF_ST))[t0 + tid] = u32x2{0xffffffffu, 0xffffffffu};
      else if (tid < 48) ((u32x4*)(ws + OFF_FL))[rt * 16 + (tid - 32)] = u32x4{0, 0, 0, 0};
    }
    MARK(0);
    float sa = 0.f, sb = 0.f, qa = 0.f, qb = 0.f;
#pragma unroll
    for (int i = 0; i < 9; ++i) {
      sa = dot2bf(v[i][0], 0x3f803f80u, sa); qa = dot2bf(v[i][0], v[i][0], qa);
      sb = dot2bf(v[i][1], 0x3f803f80u, sb); qb = dot2bf(v[i][1], v[i][1], qb);
      sa = dot2bf(v[i][2], 0x3f803f80u, sa); qa = dot2bf(v[i][2], v[i][2], qa);
      sb = dot2bf(v[i][3], 0x3f803f80u, sb); qb = dot2bf(v[i][3], v[i][3], qb);
    }
    float sm_ = sa + sb, q = qa + qb;
    sm_ += dpp<0xb1>(sm_); q += dpp<0xb1>(q);
    sm_ += dpp<0x4e>(sm_); q += dpp<0x4e>(q);
    sm_ += dpp<0x141>(sm_); q += dpp<0x141>(q);
    const float mean = sm_ * (1.f / D);
    const float var = fmaxf(q * (1.f / D) - mean * mean, 0.f);
    const float rstd = __builtin_amdgcn_rsqf(var + 1e-5f), nmr = -mean * rstd;
    MARK(1);
    constexpr int RS = 304;
    char* xs = smem + row * RS;
    char* us = smem + 32 * RS + (31 - row) * RS;
    u32x4 u0, u1, u2;
#pragma unroll
    for (int e = 0; e < 4; ++e) {
      u0[e] = pack_bf(bf_lo(s0[e]) * rstd + nmr, bf_hi(s0[e]) * rstd + nmr);
      u1[e] = pack_bf(bf_lo(s1[e]) * rstd + nmr, bf_hi(s1[e]) * rstd + nmr);
      u2[e] = pack_bf(bf_lo(s2[e]) * rstd + nmr, bf_hi(s2[e]) * rstd + nmr);
    }
    *(u32x4*)(xs + 16 * j) = s0;
    *(u32x4*)(xs + 128 + 16 * j) = s1;
    *(u32x4*)(us + 16 * j) = u0;
    *(u32x4*)(us + 128 + 16 * j) = u1;
    if (j < 2) {
      *(u32x4*)(xs + 256 + 16 * j) = s2;
      *(u32x4*)(us + 256 + 16 * j) = u2;
    }
    MARK(2);
    __syncthreads();
    MARK(3);
    const int g = lane >> 4, i16 = lane & 15, qq = i16 >> 2, pp = i16 & 3;
    const char LDSP* base = (const char LDSP*)smem + (8 * g + qq) * RS + pp * 8;
    for (int task = w; task < 18; task += 4) {
      const int dg = task >> 1, arr = task & 1;
      const int dd = 144 * cs + 16 * dg + i16;
      const char LDSP* p = base + arr * 32 * RS + dg * 32;
      bf16_t* dst = arr ? (bf16_t*)(ws + OFF_UR) + (size_t)dd * L + (2016 - t0) + 8 * g
                        : (bf16_t*)(ws + OFF_XT) + (size_t)dd * L + t0 + 8 * g;
      st16(dst, __builtin_bit_cast(u32x4, cat8(tr_read(p), tr_read(p + 4 * RS))));
    }
  }
}

constexpr int CV_S = 4384;
constexpr int CV_K = 8 * CV_S;
constexpr int CV_LDS = CV_K + 4352;

constexpr int NB_CS = 18 * 8;
__global__ __launch_bounds__(256) void k_conv(const bf16_t* __restrict__ dskip, const bf16_t* __restrict__ w1,
                                              const bf16_t* __restrict__ w3, char* __restrict__ ws) {
  __shared__ __attribute__((aligned(16))) char smem[CV_LDS];
  STAMP(1);
  const int tid = threadIdx.x, lane = tid & 63, w = tid >> 6;
  if (blockIdx.x < NB_CS) {
    const int cblk = blockIdx.x % 18, rblk = blockIdx.x / 18;
    const bf16_t* W = cblk < 9 ? w1 : w3;
    const int c0 = (cblk % 9) * 512 + lane * 8;
    const int r0 = rblk * 72 + w * 18;
    u32x4 v[18];
#pragma unroll
    for (int i = 0; i < 18; ++i) v[i] = *(const u32x4*)(W + (size_t)(r0 + i) * F + c0);
    float s[8] = {};
#pragma unroll
    for (int i = 0; i < 18; ++i)
#pragma unroll
      for (int e = 0; e < 4; ++e) { s[2 * e] += bf_lo(v[i][e]); s[2 * e + 1] += bf_hi(v[i][e]); }
    f32x4* red = (f32x4*)smem;
    red[w * 128 + lane * 2] = f32x4{s[0], s[1], s[2], s[3]};
    red[w * 128 + lane * 2 + 1] = f32x4{s[4], s[5], s[6], s[7]};
    __syncthreads();
    if (tid < 128) {
      f32x4 t = (red[tid] + red[128 + tid]) + (red[256 + tid] + red[384 + tid]);
      *(f32x4*)((float*)(ws + OFF_CS) + rblk * 9216 + cblk * 512 + tid * 4) = t;
    }
    return;
  }
  const int d = blockIdx.x - NB_CS;
  const bf16_t* ur = (const bf16_t*)(ws + OFF_UR) + (size_t)d * L;
  const bf16_t* xt = (const bf16_t*)(ws + OFF_XT) + (size_t)d * L;
  const bf16_t* kg = (const bf16_t*)(ws + OFF_KG) + (size_t)d * L;
  u32x4 c0 = *(const u32x4*)(ur + tid * 8);
  u32x4 c1 = {0, 0, 0, 0};
  if (tid < 255) c1 = *(const u32x4*)(ur + tid * 8 + 8);
  u32x4 kk = *(const u32x4*)(kg + tid * 8);
  const int m16 = lane & 15, q4 = lane >> 4;
  const int tl = 128 * (q4 >> 1) + 8 * m16 + 4 * (q4 & 1);
  u32x2 xe[2], ue[2];
#pragma unroll
  for (int i = 0; i < 2; ++i) {
    const int t = 256 * (2 * w + i) + tl;
    xe[i] = *(const u32x2*)(xt + t);
    ue[i] = *(const u32x2*)(ur + (2044 - t));
  }
  const float dsk = bf2f(dskip[d]);
  MARK(0);
  {
    unsigned e[8] = {c0[0], c0[1], c0[2], c0[3], c1[0], c1[1], c1[2], c1[3]};
    unsigned o[7];
#pragma unroll
    for (int i = 0; i < 7; ++i) o[i] = __builtin_amdgcn_alignbyte(e[i + 1], e[i], 2);
#pragma unroll
    for (int s = 0; s < 8; ++s) {
      u32x4 v;
      if (s & 1) v = u32x4{o[s >> 1], o[(s >> 1) + 1], o[(s >> 1) + 2], o[(s >> 1) + 3]};
      else v = u32x4{e[s >> 1], e[(s >> 1) + 1], e[(s >> 1) + 2], e[(s >> 1) + 3]};
      *(u32x4*)(smem + (7 - s) * CV_S + tid * 16) = v;
    }
    if (tid < 128) *(u32x4*)(smem + (tid >> 4) * CV_S + 4096 + (tid & 15) * 16) = u32x4{0, 0, 0, 0};
    *(u32x4*)(smem + CV_K + 256 + tid * 16) = kk;
    if (tid < 16) *(u32x4*)(smem + CV_K + tid * 16) = u32x4{0, 0, 0, 0};
  }
  __syncthreads();
  MARK(2);
  f32x4 acc[8] = {};
  {
    const int phi = m16 & 7, nn = m16 >> 3;
    const char* ab = smem + phi * CV_S + 2 * (1920 + 8 * q4 - 128 * nn);
    const char* bb = smem + CV_K + 2 * (8 * m16 + 8 + 8 * q4);
#pragma unroll
    for (int k2 = 0; k2 < 2; ++k2) {
      const int ks = 2 * w + k2;
      bf16x8 A[8], B[8];
#pragma unroll
      for (int g = 0; g < 8; ++g) {
        A[g] = *(const bf16x8*)(ab + 64 * ks - 512 * g);
        B[g] = *(const bf16x8*)(bb + 64 * ks + 512 * g);
      }
#pragma unroll
      for (int g = 0; g < 8; ++g)
#pragma unroll
        for (int i = 0; i + g < 8; ++i)
          acc[g + i] = __builtin_amdgcn_mfma_f32_16x16x32_bf16(A[g], B[i], acc[g + i], 0, 0, 0);
    }
  }
  MARK(3);
  __syncthreads();
  f32x4* red = (f32x4*)smem;
#pragma unroll
  for (int g = 0; g < 8; ++g)
    if ((g >> 1) != w) red[(w * 8 + g) * 64 + lane] = acc[g];
  __syncthreads();
  MARK(4);
  bf16_t* ht = (bf16_t*)(ws + OFF_HT) + (size_t)d * L;
#pragma unroll
  for (int i = 0; i < 2; ++i) {
    f32x4 s = {};
#pragma unroll
    for (int g = 0; g < 8; ++g)
      if (g == 2 * w + i) s = acc[g];
#pragma unroll
    for (int ww = 0; ww < 4; ++ww)
      if (ww != w) s += red[(ww * 8 + 2 * w + i) * 64 + lane];
    const int t = 256 * (2 * w + i) + tl;
    float h0 = bf_lo(xe[i][0]) + (s[0] + bf_hi(ue[i][1]) * dsk);
    float h1 = bf_hi(xe[i][0]) + (s[1] + bf_lo(ue[i][1]) * dsk);
    float h2 = bf_lo(xe[i][1]) + (s[2] + bf_hi(ue[i][0]) * dsk);
    float h3 = bf_hi(xe[i][1]) + (s[3] + bf_lo(ue[i][0]) * dsk);
    st8(ht + t, u32x2{pack_bf(h0, h1), pack_bf(h2, h3)});
  }
}

#define WAIT_V(n) asm volatile("s_waitcnt vmcnt(%0)" ::"n"(n) : "memory")
#define BAR() do { __builtin_amdgcn_s_barrier(); asm volatile("" ::: "memory"); } while (0)
#define SCHED() __builtin_amdgcn_sched_barrier(0)
constexpr int G1_HB = 32 * 512, G1_PL = 32 * 288 + 128, G1_STAGE = G1_HB + 2 * G1_PL;
constexpr int G1_NST = 4, G1_CSOFF = G1_NST * G1_STAGE, G1_CPOFF = G1_CSOFF + 1280, G1_LDS = G1_CPOFF + 8 * 1280;

DEV void glds16(const void* g, unsigned lds) {
  unsigned keep;
  asm volatile("s_mov_b32 %0, m0\n\ts_mov_b32 m0, %2\n\ts_nop 0\n\tglobal_load_lds_dwordx4 %1, off\n\ts_mov_b32 m0, %0"
               : "=&s"(keep) : "v"(g), "s"(lds) : "memory");
}
DEV unsigned lds_addr(const void* p) { return (unsigned)(size_t)(const char LDSP*)p; }

DEV void glds16s(unsigned voff, const void* sbase, unsigned lds) {
  unsigned keep;
  asm volatile("s_mov_b32 %0, m0\n\ts_mov_b32 m0, %3\n\ts_nop 0\n\tglobal_load_lds_dwordx4 %1, %2\n\ts_mov_b32 m0, %0"
               : "=&s"(keep) : "v"(voff), "s"(sbase), "s"(lds) : "memory");
}

struct G1Ctx {
  const char* hbase;
  const char* wbase;
  unsigned hoff[2], woff[3];
  unsigned ldsh, ldsw;
  int lane;
};
template <int PART, int IDX>
DEV void g1_issue(const G1Ctx& c, int s) {
  const unsigned sb = (unsigned)(s & 3) * G1_STAGE;
  if (PART == 0) {
    glds16s(c.hoff[IDX], c.hbase + (size_t)s * (32 * L * 2), sb + c.ldsh + IDX * 1024);
  } else if (IDX < 2) {
    glds16s(c.woff[IDX], c.wbase + (size_t)s * (32 * F * 2), sb + c.ldsw + IDX * 1024);
  } else {
    if (c.lane < 16) glds16s(c.woff[2], c.wbase + (size_t)s * (32 * F * 2), sb + c.ldsw + 2048);
  }
}

template <int NT, bool RD, int GP>
DEV void g1_kstep(f32x16 (&acc)[NT][2], const bf16x8 (&Wc)[NT], const bf16x8 (&Hc)[2], bf16x8 (&Wn)[NT],
                  bf16x8 (&Hn)[2], const char LDSP* h0, const char LDSP* h1, const char LDSP* wp, const G1Ctx& c,
                  int gs) {
#pragma unroll
  for (int i = 0; i < 2 * NT; ++i) {
    acc[i >> 1][i & 1] = __builtin_amdgcn_mfma_f32_32x32x16_bf16(Wc[i >> 1], Hc[i & 1], acc[i >> 1][i & 1], 0, 0, 0);
    if (RD) {
      if (i == 0) Hn[0] = cat8(tr_read(h0), tr_read(h0 + 4 * 512));
      else if (i == 1) Wn[0] = cat8(tr_read(wp), tr_read(wp + 4 * 288));
      else if (i == 2) Hn[1] = cat8(tr_read(h1), tr_read(h1 + 4 * 512));
      else if (i - 2 < NT) Wn[i - 2] = cat8(tr_read(wp + (i - 2) * 32), tr_read(wp + 4 * 288 + (i - 2) * 32));
    }
    if (GP == 1) {
      if (i == 2 * NT - 3) g1_issue<0, 0>(c, gs);
      if (i == 2 * NT - 2) g1_issue<0, 1>(c, gs);
    } else if (GP == 2) {
      if (i == 2 * NT - 4) g1_issue<1, 0>(c, gs);
      if (i == 2 * NT - 3) g1_issue<1, 1>(c, gs);
      if (i == 2 * NT - 2) g1_issue<1, 2>(c, gs);
    } else if (GP >= 3) {
      if (i == 1) g1_issue<0, 0>(c, gs);
      if (i == 2) g1_issue<0, 1>(c, gs);
      if (i == 3) g1_issue<1, 0>(c, gs);
      if (i == 4) g1_issue<1, 1>(c, gs);
      if (i == 5) g1_issue<1, 2>(c, gs);
      if (GP == 7 && i == 6) g1_issue<0, 0>(c, gs + 1);
      if (GP == 7 && i == 7) g1_issue<0, 1>(c, gs + 1);
    }
    SCHED();
  }
}
#define WAIT_VL(n) asm volatile("s_waitcnt vmcnt(%0) lgkmcnt(0)" ::"n"(n) : "memory")

DEV unsigned long long ld_stat(const char* ws, int t) {
  return __hip_atomic_load((const unsigned long long*)(ws + OFF_ST) + t, __ATOMIC_RELAXED, __HIP_MEMORY_SCOPE_AGENT);
}
DEV unsigned long long row_stat_of(float S, float Q) {
  const float mean = S * (1.f / D);
  const float var = fmaxf(Q * (1.f / D) - mean * mean, 0.f);
  unsigned rb = __float_as_uint(__builtin_amdgcn_rsqf(var + 1e-5f));
  if (rb == 0xffffffffu) rb = 0x7fffffffu;
  return ((unsigned long long)rb << 32) | __float_as_uint(mean);
}

template <int NT, int N0>
DEV void gemm1_body(char* smem, char* ws, const G1Ctx& c, int w, int lane, int t0, int f0 STAMP_ARG) {
  const int tw = w & 3;
  const int gq = lane >> 4, i16 = lane & 15, qq = i16 >> 2, pp = i16 & 3, hk = gq >> 1, g1 = gq & 1;
  const char LDSP* sm = (const char LDSP*)smem;
  const char LDSP* wl = sm + G1_HB + g1 * G1_PL + (8 * hk + qq) * 288 + (((pp & 1) << 1) | (pp >> 1)) * 8 + N0 * 32;
  const char LDSP* hl0 = sm + (8 * hk + qq) * 512 + (((2 * tw) ^ qq) * 64) + g1 * 32 + pp * 8;
  const char LDSP* hl1 = sm + (8 * hk + qq) * 512 + (((2 * tw + 1) ^ qq) * 64) + g1 * 32 + pp * 8;

  f32x16 acc[NT][2] = {};
  bf16x8 Wa[NT], Ha[2], Wb[NT], Hb[2];
  MARK(0);
  WAIT_V(5);
  BAR();
  MARK(1);
  Ha[0] = cat8(tr_read(hl0), tr_read(hl0 + 4 * 512));
  Ha[1] = cat8(tr_read(hl1), tr_read(hl1 + 4 * 512));
#pragma unroll
  for (int n = 0; n < NT; ++n) Wa[n] = cat8(tr_read(wl + n * 32), tr_read(wl + 4 * 288 + n * 32));
  if (N0 == 0 || w == 4) {
    const float* cp = (const float*)(smem + G1_CPOFF) + w * 64 + lane;
    ((float*)(smem + G1_CSOFF))[w * 64 + lane] =
        ((cp[0] + cp[320]) + (cp[640] + cp[960])) + ((cp[1280] + cp[1600]) + (cp[1920] + cp[2240]));
  }
  SCHED();
  g1_kstep<NT, true, 3>(acc, Wa, Ha, Wb, Hb, hl0 + 16 * 512, hl1 + 16 * 512, wl + 16 * 288, c, 2);
  WAIT_VL(5);
  BAR();
  g1_kstep<NT, true, 7>(acc, Wb, Hb, Wa, Ha, hl0 + G1_STAGE, hl1 + G1_STAGE, wl + G1_STAGE, c, 3);
#pragma unroll 1
  for (int s = 1; s < 14; ++s) {
    const int o0 = (s & 3) * G1_STAGE, o1 = ((s + 1) & 3) * G1_STAGE;
    g1_kstep<NT, true, 2>(acc, Wa, Ha, Wb, Hb, hl0 + o0 + 16 * 512, hl1 + o0 + 16 * 512, wl + o0 + 16 * 288, c, s + 3);
    WAIT_VL(10);
    BAR();
    g1_kstep<NT, true, 1>(acc, Wb, Hb, Wa, Ha, hl0 + o1, hl1 + o1, wl + o1, c, s + 4);
  }
  const int c31 = lane & 31, h = lane >> 5;
  const int tr0 = t0 + 64 * tw + c31;
  unsigned long long st0 = 0, st1 = 0;
#pragma unroll
  for (int s = 14; s < 18; ++s) {
    const int o0 = (s & 3) * G1_STAGE, o1 = ((s + 1) & 3) * G1_STAGE;
    if (s == 14)
      g1_kstep<NT, true, 2>(acc, Wa, Ha, Wb, Hb, hl0 + o0 + 16 * 512, hl1 + o0 + 16 * 512, wl + o0 + 16 * 288, c, s + 3);
    else
      g1_kstep<NT, true, 0>(acc, Wa, Ha, Wb, Hb, hl0 + o0 + 16 * 512, hl1 + o0 + 16 * 512, wl + o0 + 16 * 288, c, 0);
    if (s < 17) {
      if (s == 14) WAIT_VL(10); else if (s == 15) WAIT_VL(5); else WAIT_VL(0);
      BAR();
      if (s == 16) { st0 = ld_stat(ws, tr0); st1 = ld_stat(ws, tr0 + 32); SCHED(); }
      g1_kstep<NT, true, 0>(acc, Wb, Hb, Wa, Ha, hl0 + o1, hl1 + o1, wl + o1, c, 0);
    } else {
      g1_kstep<NT, false, 0>(acc, Wb, Hb, Wa, Ha, hl0, hl1, wl, c, 0);
    }
  }

  MARK(2);
  {
    unsigned spins = 0;
    while (__any((unsigned)(st0 >> 32) == 0xffffffffu || (unsigned)(st1 >> 32) == 0xffffffffu)) {
      if (++spins > 20000u) {
        const bf16_t* hp = (const bf16_t*)(ws + OFF_HT) + tr0;
        float S0 = 0.f, Q0 = 0.f, S1 = 0.f, Q1 = 0.f;
        for (int d = 0; d < D; ++d) {
          const float a = bf2f(hp[(size_t)d * L]), b = bf2f(hp[(size_t)d * L + 32]);
          S0 += a; Q0 += a * a; S1 += b; Q1 += b * b;
        }
        st0 = row_stat_of(S0, Q0); st1 = row_stat_of(S1, Q1);
        break;
      }
      __builtin_amdgcn_s_sleep(2);
      st0 = ld_stat(ws, tr0); st1 = ld_stat(ws, tr0 + 32);
    }
  }
  MARK(3);
  const float* csl = (const float*)(smem + G1_CSOFF);
  char* hm = ws + OFF_HM;
#pragma unroll
  for (int mt = 0; mt < 2; ++mt) {
    const int t = tr0 + 32 * mt;
    const unsigned long long st = mt ? st1 : st0;
    const float mu = __uint_as_float((unsigned)st), rs = __uint_as_float((unsigned)(st >> 32));
    const float nmu = -mu, sc = -1.44269504088896341f * rs, ir2 = __builtin_amdgcn_rcpf(rs * rs);
#pragma unroll
    for (int n = 0; n < NT; ++n) {
      const int fl = 16 * (N0 + n) + 8 * h;
      f32x4 c1a = *(const f32x4*)(csl + fl), c1b = *(const f32x4*)(csl + fl + 4);
      f32x4 c3a = *(const f32x4*)(csl + 160 + fl), c3b = *(const f32x4*)(csl + 160 + fl + 4);
      float o[8];
#pragma unroll
      for (int e = 0; e < 8; ++e) {
        float g = acc[n][mt][e] + nmu * (e < 4 ? c1a[e & 3] : c1b[e & 3]);
        float u = acc[n][mt][8 + e] + nmu * (e < 4 ? c3a[e & 3] : c3b[e & 3]);
        float ex = __builtin_amdgcn_exp2f(g * sc);
        float den = ex * ir2 + ir2;
        o[e] = (g * u) * __builtin_amdgcn_rcpf(den);
      }
      const int f = f0 + fl;
      const size_t off = ((size_t)((t >> 4) * 144 + (f >> 5)) * 64 + (t & 15) + 16 * ((f >> 3) & 3)) * 16;
      st16(hm + off, u32x4{pack_bf(o[0], o[1]), pack_bf(o[2], o[3]), pack_bf(o[4], o[5]), pack_bf(o[6], o[7])});
    }
  }
}

__global__ __launch_bounds__(512, 2) void k_gemm1(const bf16_t* __restrict__ w1, const bf16_t* __restrict__ w3,
                                                  char* __restrict__ ws) {
  extern __shared__ __attribute__((aligned(16))) char smem[];
  const int tid = threadIdx.x, lane = tid & 63;
  const int w = __builtin_amdgcn_readfirstlane(tid >> 6);
  STAMP(3);
  const int bid = blockIdx.x, xg = bid & 7, loc = bid >> 3;
  const int mt_ = 4 * (xg & 1) + (loc & 3), nt_ = 8 * (xg >> 1) + (loc >> 2);
  const int t0 = mt_ * 256, f0 = nt_ * 144;
  const unsigned sm = lds_addr(smem);

  u32x4 hv[9];
  if (w == 7) {
    const char* p = ws + OFF_HT + (size_t)lane * (L * 2) + (t0 + 8 * nt_) * 2;
#pragma unroll
    for (int i = 0; i < 9; ++i)
      asm volatile("global_load_dwordx4 %0, %1, off" : "=v"(hv[i]) : "v"(p + (size_t)i * 64 * (L * 2)) : "memory");
  }
  G1Ctx c;
  c.lane = lane;
  c.hbase = ws + OFF_HT + t0 * 2;
  c.wbase = (const char*)(w < 4 ? w1 : w3) + f0 * 2;
  c.ldsh = sm + w * 2048;
  c.ldsw = sm + G1_HB + (w >> 2) * G1_PL + (w & 3) * 2304;
#pragma unroll
  for (int i = 0; i < 2; ++i) {
    const int k = 2 * (2 * w + i) + (lane >> 5), ch = ((lane & 31) >> 2) ^ (k & 3);
    c.hoff[i] = k * (L * 2) + ch * 64 + (lane & 3) * 16;
  }
#pragma unroll
  for (int i = 0; i < 3; ++i) {
    int o = i * 1024 + lane * 16;
    if (o >= 2304) o -= 768;
    c.woff[i] = (8 * (w & 3) + o / 288) * (F * 2) + o % 288;
  }
  g1_issue<0, 0>(c, 0); g1_issue<0, 1>(c, 0);
  g1_issue<1, 0>(c, 0); g1_issue<1, 1>(c, 0); g1_issue<1, 2>(c, 0);
  if (lane < 36) {
    glds16s(lane * 16, ws + OFF_CS + (w * 9216 + f0) * 4, sm + G1_CPOFF + w * 1280);
    glds16s(lane * 16, ws + OFF_CS + (w * 9216 + F + f0) * 4, sm + G1_CPOFF + w * 1280 + 640);
  }
  g1_issue<0, 0>(c, 1); g1_issue<0, 1>(c, 1);
  g1_issue<1, 0>(c, 1); g1_issue<1, 1>(c, 1); g1_issue<1, 2>(c, 1);
  if (w == 7) {
    asm volatile("s_waitcnt vmcnt(12)"
                 : "+v"(hv[0]), "+v"(hv[1]), "+v"(hv[2]), "+v"(hv[3]), "+v"(hv[4]), "+v"(hv[5]), "+v"(hv[6]),
                   "+v"(hv[7]), "+v"(hv[8])::"memory");
    float v[16] = {};
#pragma unroll
    for (int i = 0; i < 9; ++i)
#pragma unroll
      for (int e = 0; e < 4; ++e) {
        const float a = bf_lo(hv[i][e]), b = bf_hi(hv[i][e]);
        v[2 * e] += a; v[8 + 2 * e] += a * a; v[2 * e + 1] += b; v[9 + 2 * e] += b * b;
      }
    float x8[8], y[4];
#pragma unroll
    for (int i = 0; i < 8; ++i) {
      auto r = __builtin_amdgcn_permlane32_swap(__float_as_uint(v[i]), __float_as_uint(v[8 + i]), false, false);
      x8[i] = __uint_as_float(r[0]) + __uint_as_float(r[1]);
    }
#pragma unroll
    for (int i = 0; i < 4; ++i) {
      auto r = __builtin_amdgcn_permlane16_swap(__float_as_uint(x8[i]), __float_as_uint(x8[4 + i]), false, false);
      float z = __uint_as_float(r[0]) + __uint_as_float(r[1]);
      z += dpp<0x128>(z); z += dpp<0x124>(z); z += dpp<0x122>(z); z += dpp<0x121>(z);
      y[i] = z;
    }
    const int sel = lane & 3;
    const float z = sel == 0 ? y[0] : sel == 1 ? y[1] : sel == 2 ? y[2] : y[3];
    auto r = __builtin_amdgcn_permlane32_swap(__float_as_uint(z), __float_as_uint(z), false, false);
    const unsigned long long st = row_stat_of(__uint_as_float(r[0]), __uint_as_float(r[1]));
    if (lane < 32 && (lane & 12) == 0)
      __hip_atomic_store((unsigned long long*)(ws + OFF_ST) + t0 + 8 * nt_ + 4 * (lane >> 4) + sel, st,
                         __ATOMIC_RELAXED, __HIP_MEMORY_SCOPE_AGENT);
  }
  if (w < 4) gemm1_body<5, 0>(smem, ws, c, w, lane, t0, f0 STAMP_PASS);
  else gemm1_body<4, 5>(smem, ws, c, w, lane, t0, f0 STAMP_PASS);
}

constexpr int G2_STAGE = 64 * 288;
constexpr int G2_NST = 5, G2_LDS = G2_NST * G2_STAGE;

__global__ __launch_bounds__(256, 1) void k_gemm2(const bf16_t* __restrict__ w2, char* __restrict__ ws,
                                                  bf16_t* __restrict__ out) {
  extern __shared__ __attribute__((aligned(16))) char smem[];
  const int tid = threadIdx.x, lane = tid & 63;
  const int w = __builtin_amdgcn_readfirstlane(tid >> 6);
  STAMP(4);
  const int bid = blockIdx.x, xg = bid & 7, loc = bid >> 3;
  const int ksp = xg >> 1, mt_ = 8 * (xg & 1) + (loc & 7), nt_ = loc >> 3;
  const int t0 = mt_ * 128, d0 = nt_ * 144, k0 = ksp * 1152;
  const int tile = mt_ * 4 + nt_;

  const char* wsrc[5];
#pragma unroll
  for (int i = 0; i < 5; ++i) {
    int o = i * 1024 + lane * 16;
    if (o >= 4608) o -= 512;
    const int rho = 16 * w + o / 288, c = o % 288;
    const int k = (rho & ~12) | ((rho & 4) << 1) | ((rho & 8) >> 1);
    wsrc[i] = (const char*)w2 + (size_t)(k0 + k) * (D * 2) + d0 * 2 + c;
  }
  const char* hsrc = ws + OFF_HM + ((size_t)((t0 + 32 * w) >> 4) * 144 + (k0 >> 5)) * 1024 + lane * 16;

  bf16x8 Hf[4][2][2];
  auto issue = [&](int s) {
    const unsigned sb = lds_addr(smem) + (s % G2_NST) * G2_STAGE;
#pragma unroll
    for (int i = 0; i < 4; ++i)
      glds16(wsrc[i] + (size_t)s * (64 * D * 2), sb + w * 4608 + i * 1024);
    if (lane < 32) glds16(wsrc[4] + (size_t)s * (64 * D * 2), sb + w * 4608 + 4096);
#pragma unroll
    for (int ks = 0; ks < 2; ++ks)
#pragma unroll
      for (int tt = 0; tt < 2; ++tt) {
        const char* p = hsrc + (size_t)tt * (144 * 1024) + (2 * s + ks) * 1024;
        asm volatile("global_load_dwordx4 %0, %1, off" : "=v"(Hf[s & 3][ks][tt]) : "v"(p) : "memory");
      }
  };
  issue(0); issue(1); issue(2);

  const int kq = lane >> 4, i16 = lane & 15, qq = i16 >> 2, pp = i16 & 3;
  const int wlane = (16 * (kq >> 1) + 4 * (kq & 1) + qq) * 288 + pp * 8;

  f32x4 acc[9][2] = {};
  bf16x8 Wf[2][9];
  auto load_frags = [&](int buf, int s, int ksub) {
    const char LDSP* sb = (const char LDSP*)smem + (s % G2_NST) * G2_STAGE + wlane + ksub * 32 * 288;
#pragma unroll
    for (int n = 0; n < 9; ++n) Wf[buf][n] = cat8(tr_read(sb + n * 32), tr_read(sb + 8 * 288 + n * 32));
  };
  auto mma = [&](int buf, int s, int ksub) {
#pragma unroll
    for (int n = 0; n < 9; ++n) {
      acc[n][0] = __builtin_amdgcn_mfma_f32_16x16x32_bf16(Wf[buf][n], Hf[s & 3][ksub][0], acc[n][0], 0, 0, 0);
      acc[n][1] = __builtin_amdgcn_mfma_f32_16x16x32_bf16(Wf[buf][n], Hf[s & 3][ksub][1], acc[n][1], 0, 0, 0);
    }
  };
#define PIN_H(s)                                                                                       \
  asm volatile("" : "+v"(Hf[(s) & 3][0][0]), "+v"(Hf[(s) & 3][0][1]), "+v"(Hf[(s) & 3][1][0]), \
               "+v"(Hf[(s) & 3][1][1]))
  constexpr int NST = 18;
  MARK(0);
  WAIT_V(18);
  PIN_H(0);
  BAR();
  MARK(1);
  load_frags(0, 0, 0);
#pragma unroll
  for (int s = 0; s < NST; ++s) {
    if (s + 1 < NST) {
      if (s + 2 < NST) WAIT_V(9); else WAIT_V(0);
      PIN_H(s + 1);
      BAR();
    }
    if (s + 3 < NST) issue(s + 3);
    load_frags(1, s, 1);
    mma(0, s, 0);
    if (s + 1 < NST) load_frags(0, s + 1, 0);
    mma(1, s, 1);
    SCHED();
  }

  MARK(2);
  auto rs = __builtin_amdgcn_make_buffer_rsrc(ws + OFF_SL, 0, 64 * 4 * 4 * 18 * 1024, 0x00020000);
  const int lo = ksp < 2 ? 5 * ksp : 10 + 4 * (ksp - 2), cnt = ksp < 2 ? 5 : 4;
  const unsigned slab_w = (unsigned)(((tile * 4 + ksp) * 4 + w) * 18) * 1024u + lane * 16;
#pragma unroll
  for (int it = 0; it < 18; ++it) {
    if (it < lo || it >= lo + cnt)
      __builtin_amdgcn_raw_buffer_store_b128(__builtin_bit_cast(u32x4, acc[it >> 1][it & 1]), rs, slab_w + it * 1024, 0, 16);
  }
  asm volatile("s_waitcnt vmcnt(0)" ::: "memory");
  unsigned* flags = (unsigned*)(ws + OFF_FL) + (tile * 4 + w) * 16;
  if (lane == 0) __hip_atomic_store(flags + ksp, 1u, __ATOMIC_RELAXED, __HIP_MEMORY_SCOPE_AGENT);
  MARK(3);
  const bf16_t* hT = (const bf16_t*)(ws + OFF_HT);
  float hres[5][4];
#pragma unroll
  for (int j = 0; j < 5; ++j) {
    const int it = lo + (j < cnt ? j : 0);
    const int t = t0 + 32 * w + 16 * (it & 1) + i16, dd = d0 + 16 * (it >> 1) + 4 * kq;
#pragma unroll
    for (int e = 0; e < 4; ++e) hres[j][e] = bf2f(hT[(size_t)(dd + e) * L + t]);
  }
  {
    unsigned spins = 0;
    while (true) {
      unsigned v = 1;
      if (lane < 4) v = __hip_atomic_load(flags + lane, __ATOMIC_RELAXED, __HIP_MEMORY_SCOPE_AGENT);
      if (__all(v != 0)) break;
      if (++spins > (1u << 22)) break;
      __builtin_amdgcn_s_sleep(1);
    }
  }
  __builtin_amdgcn_fence(__ATOMIC_ACQUIRE, "wavefront");
  MARK(4);
  f32x4 part[5][3];
#pragma unroll
  for (int j = 0; j < 5; ++j)
#pragma unroll
    for (int o = 0; o < 3; ++o) {
      const int src = o + (o >= ksp);
      const int it = lo + (j < cnt ? j : 0);
      const unsigned off = (unsigned)(((tile * 4 + src) * 4 + w) * 18 + it) * 1024u + lane * 16;
      part[j][o] = __builtin_bit_cast(f32x4, __builtin_amdgcn_raw_buffer_load_b128(rs, off, 0, 16));
    }
#pragma unroll
  for (int j = 0; j < 5; ++j) {
    if (j < cnt) {
      const int it = lo + j;
      f32x4 own = {};
#pragma unroll
      for (int i2 = 0; i2 < 18; ++i2)
        if (i2 == it) own = acc[i2 >> 1][i2 & 1];
      f32x4 p0 = ksp == 0 ? own : part[j][0];
      f32x4 p1 = ksp == 1 ? own : (ksp < 1 ? part[j][0] : part[j][1]);
      f32x4 p2 = ksp == 2 ? own : (ksp < 2 ? part[j][1] : part[j][2]);
      f32x4 p3 = ksp == 3 ? own : part[j][2];
      f32x4 s = (p0 + p1) + (p2 + p3);
      const int t = t0 + 32 * w + 16 * (it & 1) + i16, dd = d0 + 16 * (it >> 1) + 4 * kq;
      u32x2 o = {pack_bf(hres[j][0] + s[0], hres[j][1] + s[1]), pack_bf(hres[j][2] + s[2], hres[j][3] + s[3])};
      *(u32x2*)(out + (size_t)t * D + dd) = o;
    }
  }
}

extern "C" void kernel_launch(void* const* d_in, const int* in_sizes, int n_in, void* d_out, int out_size,
                              void* d_ws, size_t ws_size, hipStream_t stream) {
  const bf16_t* x = (const bf16_t*)d_in[0];
  const bf16_t* Are = (const bf16_t*)d_in[1];
  const bf16_t* Aim = (const bf16_t*)d_in[2];
  const bf16_t* Bre = (const bf16_t*)d_in[3];
  const bf16_t* Bim = (const bf16_t*)d_in[4];
  const bf16_t* Cre = (const bf16_t*)d_in[5];
  const bf16_t* Cim = (const bf16_t*)d_in[6];
  const bf16_t* logdt = (const bf16_t*)d_in[7];
  const bf16_t* dskip = (const bf16_t*)d_in[8];
  const bf16_t* w1 = (const bf16_t*)d_in[10];
  const bf16_t* w3 = (const bf16_t*)d_in[11];
  const bf16_t* w2 = (const bf16_t*)d_in[12];
  char* ws = (char*)d_ws;
  (void)hipFuncSetAttribute((const void*)k_gemm1, hipFuncAttributeMaxDynamicSharedMemorySize, G1_LDS);
  (void)hipFuncSetAttribute((const void*)k_gemm2, hipFuncAttributeMaxDynamicSharedMemorySize, G2_LDS);
  k_prep<<<NB_KG + NB_LN, 256, 0, stream>>>(x, Are, Aim, Bre, Bim, Cre, Cim, logdt, ws);
  k_conv<<<NB_CS + D, 256, 0, stream>>>(dskip, w1, w3, ws);
  k_gemm1<<<256, 512, G1_LDS, stream>>>(w1, w3, ws);
  k_gemm2<<<256, 256, G2_LDS, stream>>>(w2, ws, (bf16_t*)d_out);
}
```

```cpp
#include <hip/hip_runtime.h>
#include <hip/hip_bf16.h>
#include <stdint.h>

typedef __bf16 bf16_t;
typedef __attribute__((ext_vector_type(2))) __bf16 bf16x2;
typedef __attribute__((ext_vector_type(4))) __bf16 bf16x4;
typedef __attribute__((ext_vector_type(8))) __bf16 bf16x8;
typedef __attribute__((ext_vector_type(2))) float f32x2;
typedef __attribute__((ext_vector_type(4))) float f32x4;
typedef __attribute__((ext_vector_type(16))) float f32x16;
typedef __attribute__((ext_vector_type(4))) unsigned u32x4;
typedef __attribute__((ext_vector_type(2))) unsigned u32x2;
#define LDSP __attribute__((address_space(3)))
#define GLBP __attribute__((address_space(1)))
#define DEV __device__ __forceinline__
#define SCHED() __builtin_amdgcn_sched_barrier(0)

constexpr int HMS = 144, WTS = 144;
constexpr int L = 2048, D = 576, NS = 128, F = 4608;
constexpr size_t SZ_LD = (size_t)L * D * 2;
constexpr size_t OFF_XT = 0, OFF_UR = SZ_LD, OFF_HT = 3 * SZ_LD, OFF_CS = 4 * SZ_LD;
constexpr size_t OFF_ST = OFF_CS + 16 * 9216 * 4;
constexpr size_t OFF_FL = OFF_ST + 2048 * 8;
constexpr size_t OFF_EP = OFF_FL + 32768;
constexpr size_t OFF_W2T = (size_t)10 << 20;
constexpr size_t OFF_HM = (size_t)16 << 20;
constexpr size_t OFF_SL = (size_t)40 << 20;

DEV float bf_lo(unsigned v) { return __uint_as_float(v << 16); }
DEV float bf_hi(unsigned v) { return __uint_as_float(v & 0xffff0000u); }
DEV unsigned pack_bf(float a, float b) {
  bf16x2 r = {(__bf16)a, (__bf16)b};
  return __builtin_bit_cast(unsigned, r);
}
DEV void st16(void* p, u32x4 v) {
  asm volatile("global_store_dwordx4 %0, %1, off sc1\n\ts_nop 1" ::"v"(p), "v"(v) : "memory");
}
DEV void asm_store4(void* p, unsigned v, bool pred) {
  if (pred) asm volatile("global_store_dword %0, %1, off sc1" ::"v"(p), "v"(v) : "memory");
}
DEV float bf2f(bf16_t v) { return (float)v; }
DEV bf16x4 tr_read(const char LDSP* p) {
  return __builtin_amdgcn_ds_read_tr16_b64_v4bf16((bf16x4 LDSP*)p);
}
DEV bf16x8 cat8(bf16x4 a, bf16x4 b) { return __builtin_shufflevector(a, b, 0, 1, 2, 3, 4, 5, 6, 7); }

constexpr int NB_LN = 256;
typedef __attribute__((ext_vector_type(2))) _Float16 f16x2;
typedef __attribute__((ext_vector_type(8))) _Float16 f16x8;

DEV void cexp_rev(float ar, float ai, float pos, float& re, float& im) {
  float e = __builtin_amdgcn_exp2f(ar * pos);
  float th = __builtin_amdgcn_fractf(ai * pos);
  re = e * __builtin_amdgcn_cosf(th);
  im = e * __builtin_amdgcn_sinf(th);
}
DEV unsigned pack_h(float a, float b) {
  f16x2 r = {(_Float16)a, (_Float16)b};
  return __builtin_bit_cast(unsigned, r);
}
template <int CTRL>
DEV float dpp(float x) {
  return __builtin_bit_cast(float, __builtin_amdgcn_update_dpp(0, __builtin_bit_cast(int, x), CTRL, 0xf, 0xf, true));
}
DEV float wave_max(float x) {
  x = fmaxf(x, dpp<0x128>(x));
  x = fmaxf(x, dpp<0x124>(x));
  x = fmaxf(x, dpp<0x122>(x));
  x = fmaxf(x, dpp<0x121>(x));
  auto a = __builtin_amdgcn_permlane16_swap(__float_as_uint(x), __float_as_uint(x), false, false);
  x = fmaxf(__uint_as_float(a[0]), __uint_as_float(a[1]));
  auto b = __builtin_amdgcn_permlane32_swap(__float_as_uint(x), __float_as_uint(x), false, false);
  return fmaxf(__uint_as_float(b[0]), __uint_as_float(b[1]));
}
DEV float dot2bf(unsigned a, unsigned b, float c) {
  return __builtin_amdgcn_fdot2_f32_bf16(__builtin_bit_cast(bf16x2, a), __builtin_bit_cast(bf16x2, b), c, false);
}

constexpr int RTS = 80, LNS = 4;
DEV void colsum_block(const bf16_t* __restrict__ w1, const bf16_t* __restrict__ w3, float* __restrict__ cs, int cb,
                      char* smem) {
  const int tid = threadIdx.x;
  const int rblk = cb / 72, c72 = cb - rblk * 72, mat = c72 >= 36, cblk = c72 - 36 * mat;
  const int rs = tid >> 4, cc = tid & 15;
  const bf16_t* p = (mat ? w3 : w1) + (size_t)(rblk * 96 + rs) * F + cblk * 128 + cc * 8;
  u32x4 v[6];
#pragma unroll
  for (int i = 0; i < 6; ++i) v[i] = *(const u32x4*)(p + (size_t)i * 16 * F);
  float s[8] = {};
#pragma unroll
  for (int i = 0; i < 6; ++i)
#pragma unroll
    for (int e = 0; e < 4; ++e) { s[2 * e] += bf_lo(v[i][e]); s[2 * e + 1] += bf_hi(v[i][e]); }
  f32x4* red = (f32x4*)smem;
  red[rs * 32 + cc * 2] = f32x4{s[0], s[1], s[2], s[3]};
  red[rs * 32 + cc * 2 + 1] = f32x4{s[4], s[5], s[6], s[7]};
  __syncthreads();
  if (tid < 32) {
    f32x4 t = red[tid];
#pragma unroll
    for (int i = 1; i < 16; ++i) t += red[i * 32 + tid];
    st16(cs + rblk * 9216 + c72 * 128 + tid * 4, __builtin_bit_cast(u32x4, t));
  }
}

DEV void code_touch_lds(int line, unsigned lds) {
  unsigned long long pc;
  unsigned len, keep;
  asm volatile("s_getpc_b64 %0\n.Lct%=:\n\ts_mov_b32 %1, mlp_code_end-.Lct%=" : "=s"(pc), "=s"(len));
  unsigned off = (unsigned)line * 64u;
  if (off + 4u > len) off = 0;
  asm volatile("s_mov_b32 %0, m0\n\ts_mov_b32 m0, %3\n\ts_nop 0\n\tglobal_load_lds_dword %1, %2\n\ts_mov_b32 m0, %0"
               : "=&s"(keep) : "v"(off), "s"(pc), "s"(lds) : "memory");
}
DEV unsigned lds_addr(const void* p) { return (unsigned)(size_t)(const char LDSP*)p; }
DEV void glds16s(unsigned voff, const void* sbase, unsigned lds) {
  asm volatile("s_mov_b32 m0, %2\n\ts_nop 0\n\tglobal_load_lds_dwordx4 %0, %1" ::"v"(voff), "s"(sbase), "s"(lds) : "memory");
}
constexpr int CV_S = 4384;
constexpr int CV_K = 8 * CV_S;
constexpr int CV_LDS = CV_K + 4352;
constexpr int HO_LN = 64, HO_SEEN = HO_LN + 256 * 8;
constexpr int FB_CH = NB_LN, FB_CLOSE = FB_CH + D, FB_RT = FB_CLOSE + 1, FB_CS = FB_RT + 288, FB_N = FB_CS + 432;

__global__ __launch_bounds__(256, 4) void k_front(const bf16_t* __restrict__ x, const bf16_t* __restrict__ Are,
                                                  const bf16_t* __restrict__ Aim, const bf16_t* __restrict__ Bre,
                                                  const bf16_t* __restrict__ Bim, const bf16_t* __restrict__ Cre,
                                                  const bf16_t* __restrict__ Cim, const bf16_t* __restrict__ logdt,
                                                  const bf16_t* __restrict__ dskip, const bf16_t* __restrict__ w1,
                                                  const bf16_t* __restrict__ w3, const bf16_t* __restrict__ w2,
                                                  char* __restrict__ ws) {
  __shared__ __attribute__((aligned(16))) char smem[CV_LDS];
  const int tid = threadIdx.x, lane = tid & 63, w = tid >> 6;
  const int b = blockIdx.x < D ? blockIdx.x + FB_CH : blockIdx.x < D + FB_CH ? blockIdx.x - D : blockIdx.x;
  unsigned* hand = (unsigned*)(ws + OFF_EP);
  if (b >= FB_CS) {
    colsum_block(w1, w3, (float*)(ws + OFF_CS), b - FB_CS, smem);
    return;
  }
  if (b >= FB_RT) {
    __builtin_amdgcn_s_sleep(RTS);
    const int sl = b - FB_RT, pks = sl >> 1, phf = sl & 1;
    constexpr int RS = 576 + 32;
    const char* src = (const char*)(w2 + (size_t)pks * 32 * D + phf * 288);
    u32x4 v[5];
#pragma unroll
    for (int i = 0; i < 5; ++i) {
      int c = tid + 256 * i;
      if (c >= 1152) c -= 128;
      const int row = c / 36;
      v[i] = *(const u32x4*)(src + row * (D * 2) + (c - row * 36) * 16);
    }
#pragma unroll
    for (int i = 0; i < 5; ++i) {
      int c = tid + 256 * i;
      if (c >= 1152) c -= 128;
      const int row = c / 36;
      *(u32x4*)(smem + row * RS + (c - row * 36) * 16) = v[i];
    }
    __syncthreads();
    const char LDSP* p = (const char LDSP*)smem + (8 * (lane >> 4) + ((lane & 15) >> 2)) * RS + (lane & 3) * 8;
    for (int n = w; n < 18; n += 4)
      st16(ws + OFF_W2T + ((size_t)(phf * 18 + n) * WTS + pks) * 1024 + lane * 16,
           __builtin_bit_cast(u32x4, cat8(tr_read(p + n * 32), tr_read(p + 4 * RS + n * 32))));
    return;
  }
  unsigned tag;
  if (b == FB_CLOSE) {
    tag = __hip_atomic_load(hand, __ATOMIC_RELAXED, __HIP_MEMORY_SCOPE_AGENT) + 1u;
    unsigned spins = 0;
    for (int i = tid; i < 256 + D; i += 256) {
      const unsigned* p = hand + (i < 256 ? HO_LN + i * 8 : HO_SEEN + i - 256);
      while (__hip_atomic_load(p, __ATOMIC_RELAXED, __HIP_MEMORY_SCOPE_AGENT) != tag && ++spins < (1u << 15))
        __builtin_amdgcn_s_sleep(32);
    }
    __syncthreads();
    if (tid == 0) __hip_atomic_store(hand, tag, __ATOMIC_RELAXED, __HIP_MEMORY_SCOPE_AGENT);
    return;
  }
  if (b < FB_CH) {
    __builtin_amdgcn_s_sleep(LNS);
    __builtin_amdgcn_s_setprio(3);
    const int rt = (b & 7) * 8 + (b >> 5), cs = (b >> 3) & 3;
    const int t0 = rt * 32, row = tid >> 3, j = tid & 7;
    const u32x4* xr = (const u32x4*)(x + (size_t)(t0 + row) * D);
    u32x4 v[9];
#pragma unroll
    for (int i = 0; i < 9; ++i) v[i] = xr[j + 8 * i];
    const u32x4 s0 = xr[18 * cs + j], s1 = xr[18 * cs + 8 + j], s2 = xr[18 * cs + 16 + (j & 1)];
    if (cs == 0) {
      if (tid < 32) ((u32x2*)(ws + OFF_ST))[t0 + tid] = u32x2{0xffffffffu, 0xffffffffu};
      else if (tid < 36) ((unsigned*)(ws + OFF_FL))[(rt * 4 + tid - 32) * 32] = 0;
    }
    tag = __hip_atomic_load(hand, __ATOMIC_RELAXED, __HIP_MEMORY_SCOPE_AGENT);
    SCHED();
    float sa = 0.f, sb = 0.f, qa = 0.f, qb = 0.f;
#pragma unroll
    for (int i = 0; i < 9; ++i) {
      sa = dot2bf(v[i][0], 0x3f803f80u, sa); qa = dot2bf(v[i][0], v[i][0], qa);
      sb = dot2bf(v[i][1], 0x3f803f80u, sb); qb = dot2bf(v[i][1], v[i][1], qb);
      sa = dot2bf(v[i][2], 0x3f803f80u, sa); qa = dot2bf(v[i][2], v[i][2], qa);
      sb = dot2bf(v[i][3], 0x3f803f80u, sb); qb = dot2bf(v[i][3], v[i][3], qb);
    }
    float sm_ = sa + sb, q = qa + qb;
    sm_ += dpp<0xb1>(sm_); q += dpp<0xb1>(q);
    sm_ += dpp<0x4e>(sm_); q += dpp<0x4e>(q);
    sm_ += dpp<0x141>(sm_); q += dpp<0x141>(q);
    const float mean = sm_ * (1.f / D);
    const float var = fmaxf(q * (1.f / D) - mean * mean, 0.f);
    const float rstd = __builtin_amdgcn_rsqf(var + 1e-5f), nmr = -mean * rstd;
    constexpr int RS = 304;
    char* xs = smem + row * RS;
    char* us = smem + 32 * RS + (31 - row) * RS;
    u32x4 u0, u1, u2;
#pragma unroll
    for (int e = 0; e < 4; ++e) {
      u0[e] = pack_bf(bf_lo(s0[e]) * rstd + nmr, bf_hi(s0[e]) * rstd + nmr);
      u1[e] = pack_bf(bf_lo(s1[e]) * rstd + nmr, bf_hi(s1[e]) * rstd + nmr);
      u2[e] = pack_bf(bf_lo(s2[e]) * rstd + nmr, bf_hi(s2[e]) * rstd + nmr);
    }
    *(u32x4*)(xs + 16 * j) = s0;
    *(u32x4*)(xs + 128 + 16 * j) = s1;
    *(u32x4*)(us + 16 * j) = u0;
    *(u32x4*)(us + 128 + 16 * j) = u1;
    if (j < 2) {
      *(u32x4*)(xs + 256 + 16 * j) = s2;
      *(u32x4*)(us + 256 + 16 * j) = u2;
    }
    __syncthreads();
    const int g = lane >> 4, i16 = lane & 15, qq = i16 >> 2, pp = i16 & 3;
    const char LDSP* base = (const char LDSP*)smem + (8 * g + qq) * RS + pp * 8;
    for (int task = w; task < 18; task += 4) {
      const int dg = task >> 1, arr = task & 1;
      const int dd = 144 * cs + 16 * dg + i16;
      const char LDSP* p = base + arr * 32 * RS + dg * 32;
      bf16_t* dst = arr ? (bf16_t*)(ws + OFF_UR) + (size_t)dd * L + (2016 - t0) + 8 * g
                        : (bf16_t*)(ws + OFF_XT) + (size_t)dd * L + t0 + 8 * g;
      st16(dst, __builtin_bit_cast(u32x4, cat8(tr_read(p), tr_read(p + 4 * RS))));
    }
    asm volatile("s_waitcnt vmcnt(0)" ::: "memory");
    __syncthreads();
    if (tid == 0) __hip_atomic_store(hand + HO_LN + (cs * 64 + rt) * 8, tag + 1u, __ATOMIC_RELAXED, __HIP_MEMORY_SCOPE_AGENT);
    if (b < 32) code_touch_lds((b >> 3) * 256 + tid, lds_addr(smem) + __builtin_amdgcn_readfirstlane(w) * 256);
    return;
  }
  const int d = b - FB_CH;
  u32x4 c0, c1, xe, ue;
  const int m16 = lane & 15, q4 = lane >> 4, te = 8 * tid;
  float dskv;
  {
    f32x4* prm = (f32x4*)smem + w * 64;
    f32x4* red = (f32x4*)(smem + 4096);
    const int o = d * (NS / 2) + lane;
    const unsigned vare = ((const unsigned*)Are)[o];
    unsigned vaim = ((const unsigned*)Aim)[o];
    const unsigned vbre = ((const unsigned*)Bre)[o], vbim = ((const unsigned*)Bim)[o];
    const unsigned vcre = ((const unsigned*)Cre)[o], vcim = ((const unsigned*)Cim)[o];
    float dt = bf2f(logdt[d]);
    float dsk = bf2f(dskip[d]);
    tag = __hip_atomic_load(hand, __ATOMIC_RELAXED, __HIP_MEMORY_SCOPE_AGENT);
    asm volatile("" : "+v"(vaim), "+v"(dt), "+v"(dsk));
    dt = __expf(dt);
    float ar[2], ai[2], cr[2], ci[2];
#pragma unroll
    for (int jj = 0; jj < 2; ++jj) {
      const float are = jj ? bf_hi(vare) : bf_lo(vare), aim = jj ? bf_hi(vaim) : bf_lo(vaim);
      const float bre = dt * (jj ? bf_hi(vbre) : bf_lo(vbre)), bim = dt * (jj ? bf_hi(vbim) : bf_lo(vbim));
      const float cre = jj ? bf_hi(vcre) : bf_lo(vcre), cim = jj ? bf_hi(vcim) : bf_lo(vcim);
      ar[jj] = __builtin_amdgcn_fmed3f((dt * are) * 1.44269504088896341f, -1e30f, 1e30f);
      ai[jj] = (dt * aim) * 0.15915494309189535f;
      cr[jj] = cre * bre - cim * bim;
      ci[jj] = cre * bim + cim * bre;
    }
    const float armax = wave_max(fmaxf(fmaxf(ar[0], ar[1]), 0.f));
    const float S = wave_max(fmaxf(fmaxf(fabsf(cr[0]), fabsf(ci[0])), fmaxf(fabsf(cr[1]), fabsf(ci[1]))));
    const float inv = S > 0.f ? 0.5f * __builtin_amdgcn_rcpf(S) : 0.f;
    if ((lane >> 4) == w) {
      prm[2 * (lane & 15)] = f32x4{ar[0] - armax, ai[0], cr[0] * inv, ci[0] * inv};
      prm[2 * (lane & 15) + 1] = f32x4{ar[1] - armax, ai[1], cr[1] * inv, ci[1] * inv};
    }
    __builtin_amdgcn_wave_barrier();
    {
      const f32x4 p = prm[lane & 31];
      float re, im;
      cexp_rev(p[0], p[1], lane < 32 ? 4.f : 1024.f, re, im);
      ((f32x2*)(prm + 32))[2 * (lane & 31) + (lane >> 5)] = f32x2{re, im};
    }
    __builtin_amdgcn_wave_barrier();
    const int wu = __builtin_amdgcn_readfirstlane(w);
    tag += 1u;
    asm_store4(hand + HO_SEEN + d, tag, tid == 0);
    const int r = lane & 15, kq = lane >> 4;
    const float pf = (float)(16 * (r >> 2) + (r & 3)), pc = (float)(64 * r);
    f32x4 acc[4][2] = {};
#pragma unroll
    for (int ks = 0; ks < 2; ++ks) {
      u32x4 A[4], B[2];
#pragma unroll
      for (int e = 0; e < 4; ++e) {
        const f32x4 p = prm[16 * ks + 4 * kq + e], c = prm[32 + 16 * ks + 4 * kq + e];
        float re, im;
        cexp_rev(p[0], p[1], pf, re, im);
        A[0][e] = pack_h(re, im);
#pragma unroll
        for (int ft = 1; ft < 4; ++ft) {
          const float nr = re * c[0] - im * c[1], ni = re * c[1] + im * c[0];
          re = nr; im = ni;
          A[ft][e] = pack_h(re, im);
        }
        cexp_rev(p[0], p[1], pc, re, im);
        const float gr = p[2] * re - p[3] * im, gi = p[2] * im + p[3] * re;
        B[0][e] = pack_h(gr, -gi);
        B[1][e] = pack_h(gr * c[2] - gi * c[3], -(gr * c[3] + gi * c[2]));
      }
#pragma unroll
      for (int ft = 0; ft < 4; ++ft)
#pragma unroll
        for (int ct = 0; ct < 2; ++ct)
          acc[ft][ct] = __builtin_amdgcn_mfma_f32_16x16x32_f16(__builtin_bit_cast(f16x8, A[ft]),
                                                               __builtin_bit_cast(f16x8, B[ct]), acc[ft][ct], 0, 0, 0);
    }
#pragma unroll
    for (int ft = 0; ft < 4; ++ft)
#pragma unroll
      for (int ct = 0; ct < 2; ++ct) {
        const int item = ct * 4 + ft, ow = item >> 1;
        if (ow != w) red[((ow * 3 + ((w - ow - 1) & 3)) * 2 + (item & 1)) * 64 + lane] = acc[ft][ct];
      }
    if (wu == 0) {
      const unsigned* fp = hand + HO_LN + ((d / 144) * 64 + lane) * 8;
      unsigned spins = 0;
      while (__any(__hip_atomic_load(fp, __ATOMIC_RELAXED, __HIP_MEMORY_SCOPE_AGENT) != tag) && ++spins < (1u << 16))
        __builtin_amdgcn_s_sleep(12);
    }
    __syncthreads();
    {
      const char* ub = ws + OFF_UR + (size_t)d * L * 2;
      asm volatile("global_load_dwordx4 %0, %4, %7 sc1\n\tglobal_load_dwordx4 %1, %5, %7 sc1\n\t"
                   "global_load_dwordx4 %2, %4, %8 sc1\n\tglobal_load_dwordx4 %3, %6, %7 sc1"
                   : "=&v"(c0), "=&v"(c1), "=&v"(xe), "=&v"(ue)
                   : "v"(tid * 16), "v"(tid < 255 ? tid * 16 + 16 : 0), "v"((2040 - te) * 2), "s"(ub),
                     "s"(ws + OFF_XT + (size_t)d * L * 2) : "memory");
      SCHED();
    }
    f32x4 sv[2];
#pragma unroll
    for (int it = 0; it < 2; ++it) {
      const f32x4 c0 = acc[it][0], c1 = acc[2 + it][0], c2 = acc[it][1], c3 = acc[2 + it][1];
      f32x4 own;
#pragma unroll
      for (int e = 0; e < 4; ++e) {
        const float lo = (w & 1) ? c1[e] : c0[e], hi = (w & 1) ? c3[e] : c2[e];
        own[e] = (w & 2) ? hi : lo;
      }
      const f32x4* rp = red + (w * 6 + it) * 64 + lane;
      sv[it] = (own + rp[0]) + (rp[128] + rp[256]);
    }
    const int l = 64 * (16 * (w >> 1) + r) + 16 * kq + 8 * (w & 1);
    const float S2 = inv > 0.f ? 1.f / inv : 0.f;
    float ov[8];
#pragma unroll
    for (int e = 0; e < 8; ++e) ov[e] = sv[e >> 2][e & 3] * S2;
    if (armax > 0.f) {
#pragma unroll
      for (int e = 0; e < 8; ++e) ov[e] *= __builtin_amdgcn_exp2f(armax * (float)(l + e));
    }
    *(u32x4*)(smem + CV_K + 256 + l * 2) =
        u32x4{pack_bf(ov[0], ov[1]), pack_bf(ov[2], ov[3]), pack_bf(ov[4], ov[5]), pack_bf(ov[6], ov[7])};
    dskv = dsk;
  }
  const float dsk = dskv;
  __syncthreads();
  asm volatile("s_waitcnt vmcnt(0)" : "+v"(c0), "+v"(c1), "+v"(xe), "+v"(ue)::"memory");
  {
    if (tid == 255) c1 = u32x4{0, 0, 0, 0};
    unsigned e[8] = {c0[0], c0[1], c0[2], c0[3], c1[0], c1[1], c1[2], c1[3]};
    unsigned o[7];
#pragma unroll
    for (int i = 0; i < 7; ++i) o[i] = __builtin_amdgcn_alignbyte(e[i + 1], e[i], 2);
#pragma unroll
    for (int s = 0; s < 8; ++s) {
      u32x4 v;
      if (s & 1) v = u32x4{o[s >> 1], o[(s >> 1) + 1], o[(s >> 1) + 2], o[(s >> 1) + 3]};
      else v = u32x4{e[s >> 1], e[(s >> 1) + 1], e[(s >> 1) + 2], e[(s >> 1) + 3]};
      *(u32x4*)(smem + (7 - s) * CV_S + tid * 16) = v;
    }
    if (tid < 128) *(u32x4*)(smem + (tid >> 4) * CV_S + 4096 + (tid & 15) * 16) = u32x4{0, 0, 0, 0};
        if (tid < 16) *(u32x4*)(smem + CV_K + tid * 16) = u32x4{0, 0, 0, 0};
  }
  __syncthreads();
  SCHED();
  f32x4 ac[8] = {};
  {
    const int phi = m16 & 7, nn = m16 >> 3;
    const char* ab = smem + phi * CV_S + 2 * (1920 + 8 * q4 - 128 * nn);
    const char* bb = smem + CV_K + 2 * (8 * m16 + 8 + 8 * q4);
#pragma unroll
    for (int k2 = 0; k2 < 2; ++k2) {
      const int ks = 2 * w + k2;
      bf16x8 A[8], B[8];
#pragma unroll
      for (int g = 0; g < 8; ++g) {
        A[g] = *(const bf16x8*)(ab + 64 * ks - 512 * g);
        B[g] = *(const bf16x8*)(bb + 64 * ks + 512 * g);
      }
#pragma unroll
      for (int g = 0; g < 8; ++g)
#pragma unroll
        for (int i = 0; i + g < 8; ++i)
          ac[g + i] = __builtin_amdgcn_mfma_f32_16x16x32_bf16(A[g], B[i], ac[g + i], 0, 0, 0);
    }
  }
  __syncthreads();
  f32x4* rd2 = (f32x4*)smem;
#pragma unroll
  for (int g = 0; g < 8; ++g) rd2[(w * 8 + g) * 64 + (q4 >> 1) * 32 + m16 * 2 + (q4 & 1)] = ac[g];
  __syncthreads();
  bf16_t* ht = (bf16_t*)(ws + OFF_HT) + (size_t)d * L;
  {
    const f32x4* rp = rd2 + tid * 2;
    const f32x4 s0 = (rp[0] + rp[512]) + (rp[1024] + rp[1536]);
    const f32x4 s1 = (rp[1] + rp[513]) + (rp[1025] + rp[1537]);
    const float h0 = bf_lo(xe[0]) + (s0[0] + bf_hi(ue[3]) * dsk), h1 = bf_hi(xe[0]) + (s0[1] + bf_lo(ue[3]) * dsk);
    const float h2 = bf_lo(xe[1]) + (s0[2] + bf_hi(ue[2]) * dsk), h3 = bf_hi(xe[1]) + (s0[3] + bf_lo(ue[2]) * dsk);
    const float h4 = bf_lo(xe[2]) + (s1[0] + bf_hi(ue[1]) * dsk), h5 = bf_hi(xe[2]) + (s1[1] + bf_lo(ue[1]) * dsk);
    const float h6 = bf_lo(xe[3]) + (s1[2] + bf_hi(ue[0]) * dsk), h7 = bf_hi(xe[3]) + (s1[3] + bf_lo(ue[0]) * dsk);
    st16(ht + te, u32x4{pack_bf(h0, h1), pack_bf(h2, h3), pack_bf(h4, h5), pack_bf(h6, h7)});
  }
}

#define WAIT_V(n) asm volatile("s_waitcnt vmcnt(%0)" ::"n"(n) : "memory")
#define BAR() do { __builtin_amdgcn_s_barrier(); asm volatile("" ::: "memory"); } while (0)
constexpr int G1_HB = 32 * 512, G1_PL = 32 * 288 + 128, G1_STAGE = G1_HB + 2 * G1_PL;
constexpr int G1_NST = 4, G1_CSOFF = G1_NST * G1_STAGE, G1_CPOFF = G1_CSOFF + 1280, G1_GOFF = G1_CPOFF + 8192,
              G1_LDS = G1_GOFF + 3072 + 256;

DEV void glds16s_q(unsigned voff, const void* sbase, unsigned lds) {
  asm volatile("s_mov_b32 m0, %2\n\ts_mov_b64 exec, 0xffff\n\tglobal_load_lds_dwordx4 %0, %1\n\ts_mov_b64 exec, -1"
               ::"v"(voff), "s"(sbase), "s"(lds) : "memory");
}
constexpr int G2_K1 = 9, G1_TBAR = 2;
DEV void glds16s_h(unsigned voff, const void* sbase, unsigned lds) {
  asm volatile("s_mov_b32 m0, %2\n\ts_mov_b32 exec_hi, 0\n\tglobal_load_lds_dwordx4 %0, %1\n\ts_mov_b32 exec_hi, -1"
               ::"v"(voff), "s"(sbase), "s"(lds) : "memory");
}
struct G1Ctx {
  const char* hbase;
  const char* wbase;
  unsigned hoff[2], woff[5];
  unsigned ldsh, ldsw;
};
template <int IDX>
DEV void g1_issue(const G1Ctx& c, int s) {
  const unsigned sb = (unsigned)(s & 3) * G1_STAGE;
  s = s < 17 ? s : 17;
  if (IDX < 4) {
    glds16s(c.hoff[IDX & 1], c.hbase + (size_t)s * (32 * L * 2) + (IDX >> 1) * (4 * L * 2), sb + c.ldsh + IDX * 1024);
  } else if (IDX < 8) {
    glds16s(c.woff[IDX - 4], c.wbase + (size_t)s * (32 * F * 2), sb + c.ldsw + (IDX - 4) * 1024);
  } else {
    glds16s_h(c.woff[4], c.wbase + (size_t)s * (32 * F * 2), sb + c.ldsw + 4096);
  }
}
DEV void g1_issue_h(const G1Ctx& c, int s) { g1_issue<0>(c, s); g1_issue<1>(c, s); g1_issue<2>(c, s); g1_issue<3>(c, s); }
DEV void g1_issue_w(const G1Ctx& c, int s) {
  g1_issue<4>(c, s); g1_issue<5>(c, s); g1_issue<6>(c, s); g1_issue<7>(c, s); g1_issue<8>(c, s);
}

#define WAIT_VL(n) asm volatile("s_waitcnt vmcnt(%0) lgkmcnt(0)" ::"n"(n) : "memory")
template <int NT, int GL>
DEV void g1_kstepT(f32x16 (&acc)[5][2], const bf16x8 (&Wc)[5], const bf16x8 (&Hc)[2], bf16x8 (&Wn)[5],
                   bf16x8 (&Hn)[2], const char LDSP* h0, const char LDSP* h1, const char LDSP* wp, const G1Ctx& c,
                   int gs) {
  constexpr int R0 = GL == 1 ? G1_TBAR : 0;
#pragma unroll
  for (int i = 0; i < 2 * NT; ++i) {
    if (GL == 1 && i == G1_TBAR) { WAIT_VL(18); BAR(); }
    acc[i >> 1][i & 1] = __builtin_amdgcn_mfma_f32_32x32x16_bf16(Wc[i >> 1], Hc[i & 1], acc[i >> 1][i & 1], 0, 0, 0);
    const int r = i - R0;
    if (r == 0) Hn[0] = cat8(tr_read(h0), tr_read(h0 + 4 * 512));
    if (r == 1) Wn[0] = cat8(tr_read(wp), tr_read(wp + 4 * 288));
    if (r == 2) Hn[1] = cat8(tr_read(h1), tr_read(h1 + 4 * 512));
    if (r >= 3 && r - 2 < NT) Wn[r - 2] = cat8(tr_read(wp + (r - 2) * 32), tr_read(wp + 4 * 288 + (r - 2) * 32));
    if (NT == 4 && GL == 1) {
      if (i == 2) g1_issue<0>(c, gs);
      if (i == 4) g1_issue<1>(c, gs);
      if (i == 6) g1_issue<2>(c, gs);
      if (i == 7) g1_issue<3>(c, gs);
    } else if (NT == 4 && GL == 2) {
      if (i == 1) g1_issue<4>(c, gs);
      if (i == 3) g1_issue<5>(c, gs);
      if (i == 4) g1_issue<6>(c, gs);
      if (i == 5) g1_issue<7>(c, gs);
      if (i == 6) g1_issue<8>(c, gs);
    }
    SCHED();
  }
}
DEV void g1_kstep(f32x16 (&acc)[5][2], const bf16x8 (&Wc)[5], const bf16x8 (&Hc)[2], bf16x8 (&Wn)[5],
                  bf16x8 (&Hn)[2], const char LDSP* h0, const char LDSP* h1, const char LDSP* wp, bool five) {
#pragma unroll
  for (int i = 0; i < 8; ++i) {
    acc[i >> 1][i & 1] = __builtin_amdgcn_mfma_f32_32x32x16_bf16(Wc[i >> 1], Hc[i & 1], acc[i >> 1][i & 1], 0, 0, 0);
    if (i == 0) Hn[0] = cat8(tr_read(h0), tr_read(h0 + 4 * 512));
    if (i == 0) Wn[0] = cat8(tr_read(wp), tr_read(wp + 4 * 288));
    if (i == 1) Hn[1] = cat8(tr_read(h1), tr_read(h1 + 4 * 512));
    if (i >= 2 && i <= 4) Wn[i - 1] = cat8(tr_read(wp + (i - 1) * 32), tr_read(wp + 4 * 288 + (i - 1) * 32));
    SCHED();
  }
  if (five) {
    acc[4][0] = __builtin_amdgcn_mfma_f32_32x32x16_bf16(Wc[4], Hc[0], acc[4][0], 0, 0, 0);
    Wn[4] = cat8(tr_read(wp + 4 * 32), tr_read(wp + 4 * 288 + 4 * 32));
    acc[4][1] = __builtin_amdgcn_mfma_f32_32x32x16_bf16(Wc[4], Hc[1], acc[4][1], 0, 0, 0);
  }
  SCHED();
}

DEV unsigned long long ld_stat(const char* ws, int t) {
  return __hip_atomic_load((const unsigned long long*)(ws + OFF_ST) + t, __ATOMIC_RELAXED, __HIP_MEMORY_SCOPE_AGENT);
}
DEV unsigned long long row_stat_of(float S, float Q) {
  const float mean = S * (1.f / D);
  const float var = fmaxf(Q * (1.f / D) - mean * mean, 0.f);
  unsigned rb = __float_as_uint(__builtin_amdgcn_rsqf(var + 1e-5f));
  if (rb == 0xffffffffu) rb = 0x7fffffffu;
  return ((unsigned long long)rb << 32) | __float_as_uint(mean);
}

DEV u32x4 not_sentinel(u32x4 v) {
#pragma unroll
  for (int e = 0; e < 4; ++e) v[e] = v[e] == 0xffffffffu ? 0x7fffffffu : v[e];
  return v;
}
DEV bool is_sentinel(u32x4 v) {
  return v[0] == 0xffffffffu || v[1] == 0xffffffffu || v[2] == 0xffffffffu || v[3] == 0xffffffffu;
}
constexpr int G2_SLOT = 18432, G2_XOFF = 7 * G2_SLOT, G2_LDS = G2_XOFF + 32768;

__global__ __launch_bounds__(512, 2) void k_mlp(const bf16_t* __restrict__ w1, const bf16_t* __restrict__ w3,
                                                char* __restrict__ ws, bf16_t* __restrict__ out) {
  extern __shared__ __attribute__((aligned(16))) char smem[];
  const int tid = threadIdx.x, lane = tid & 63;
  const int w = __builtin_amdgcn_readfirstlane(tid >> 6);
  const int bid = blockIdx.x, xg = bid & 7, loc = bid >> 3;
  const int mt_ = 4 * (xg & 1) + (loc & 3), nt_ = 8 * (xg >> 1) + (loc >> 2);
  const int t0 = mt_ * 256, f0 = nt_ * 144;
  const unsigned smb = lds_addr(smem);

  const bool five = w >= 4;
  G1Ctx c;
  c.hbase = ws + OFF_HT + t0 * 2;
  c.wbase = (const char*)((w & 2) ? w3 : w1) + f0 * 2;
  c.ldsh = smb + w * 4096;
  c.ldsw = smb + G1_HB + ((w >> 1) & 1) * G1_PL + (w & 1) * 4608;
  if (!five) {
#pragma unroll
    for (int i = 0; i < 2; ++i) {
      const int k = 8 * w + 2 * i + (lane >> 5), ch = ((lane & 31) >> 2) ^ (k & 3);
      c.hoff[i] = k * (L * 2) + ch * 64 + (lane & 3) * 16;
    }
#pragma unroll
    for (int i = 0; i < 5; ++i) {
      int o = i * 1024 + lane * 16;
      if (o >= 4608) o -= 512;
      c.woff[i] = (16 * (w & 1) + o / 288) * (F * 2) + o % 288;
    }
#pragma unroll 1
    for (int s = 0; s < 2; ++s) { g1_issue_w(c, s); g1_issue_h(c, s); }
  }
  if (!five) {
#pragma unroll
    for (int i = 0; i < 2; ++i) {
      int ch = (w * 2 + i) * 64 + lane;
      if (ch > 431) ch = 431;
      const int p = ch / 72, r = ch - p * 72;
      glds16s((p * 9216 + (r >= 36 ? F - 144 : 0)) * 4 + r * 16, ws + OFF_CS + f0 * 4, smb + G1_CPOFF + (w * 2 + i) * 1024);
    }
  }
  const int tw = w & 3, N0 = five ? 4 : 0;
  const int gq = lane >> 4, i16 = lane & 15, qq = i16 >> 2, pp = i16 & 3, hk = gq >> 1, g1 = gq & 1;
  const char LDSP* sm = (const char LDSP*)smem;
  const char LDSP* wl = sm + G1_HB + g1 * G1_PL + (8 * hk + qq) * 288 + (((pp & 1) << 1) | (pp >> 1)) * 8 + N0 * 32;
  const char LDSP* hl0 = sm + (8 * hk + qq) * 512 + (((2 * tw) ^ qq) * 64) + g1 * 32 + pp * 8;
  const char LDSP* hl1 = sm + (8 * hk + qq) * 512 + (((2 * tw + 1) ^ qq) * 64) + g1 * 32 + pp * 8;

  f32x16 acc[5][2] = {};
  bf16x8 Wa[5], Ha[2], Wb[5], Hb[2];
  WAIT_V(11);
  BAR();
  Ha[0] = cat8(tr_read(hl0), tr_read(hl0 + 4 * 512));
  Ha[1] = cat8(tr_read(hl1), tr_read(hl1 + 4 * 512));
#pragma unroll
  for (int n = 0; n < 5; ++n) Wa[n] = cat8(tr_read(wl + n * 32), tr_read(wl + 4 * 288 + n * 32));
  SCHED();
  if (five) {
#pragma unroll 1
    for (int s = 0; s < 15; ++s) {
      const int o0 = (s & 3) * G1_STAGE, o1 = ((s + 1) & 3) * G1_STAGE;
      g1_kstepT<5, 2>(acc, Wa, Ha, Wb, Hb, hl0 + o0 + 16 * 512, hl1 + o0 + 16 * 512, wl + o0 + 16 * 288, c, s + 3);
      g1_kstepT<5, 1>(acc, Wb, Hb, Wa, Ha, hl0 + o1, hl1 + o1, wl + o1, c, s + 4);
    }
  } else {
    g1_issue_w(c, 2); g1_issue_h(c, 2); g1_issue_h(c, 3);
    float sv[8] = {};
    const char* sp = smem + (lane & 31) * 512 + (((nt_ >> 2) ^ (lane & 3)) * 64) + (nt_ & 3) * 16 + (lane >> 5) * 8;
    u32x2 sr = *(const u32x2*)sp;
    auto stat_add = [&](float (&a)[8], u32x2 v) {
      const float a0 = bf_lo(v[0]), a1 = bf_hi(v[0]), a2 = bf_lo(v[1]), a3 = bf_hi(v[1]);
      a[0] += a0; a[1] += a1; a[2] += a2; a[3] += a3;
      a[4] += a0 * a0; a[5] += a1 * a1; a[6] += a2 * a2; a[7] += a3 * a3;
    };
    const unsigned jobs = 0x3fcu | (w == 3 ? 0x802u : w < 2 ? 0x2000u : 0x2u);
#pragma unroll 1
    for (int s = 0; s < 15; ++s) {
      const int o0 = (s & 3) * G1_STAGE, o1 = ((s + 1) & 3) * G1_STAGE;
      g1_kstepT<4, 2>(acc, Wa, Ha, Wb, Hb, hl0 + o0 + 16 * 512, hl1 + o0 + 16 * 512, wl + o0 + 16 * 288, c, s + 3);
      g1_kstepT<4, 1>(acc, Wb, Hb, Wa, Ha, hl0 + o1, hl1 + o1, wl + o1, c, s + 4);
      stat_add(sv, sr);
      sr = *(const u32x2*)(sp + o1);
      if (__builtin_expect((jobs >> s) & 1, 0)) {
        if (s == 1) {
          if (w == 3) {
#pragma unroll
            for (int j = 0; j < 3; ++j)
              glds16s((384 + 64 * j + lane) * (L * 2) + nt_ * 16, c.hbase, smb + G1_GOFF + j * 1024);
          } else if (lane < 16) {
            code_touch_lds(loc * 16 + lane, smb + G1_GOFF + 3072);
          }
        } else if (s < 10) {
          const u32x4 ones = {0xffffffffu, 0xffffffffu, 0xffffffffu, 0xffffffffu};
          asm volatile("global_store_dwordx4 %0, %1, %2 sc1\n\ts_nop 1" ::"v"((unsigned)lane * 16), "v"(ones),
                       "s"(ws + OFF_SL + (size_t)(((8 * (xg & 1) + (loc & 7)) * 4 + (loc >> 3)) * 4 + (xg >> 1)) * 32768 +
                           (w * 8 + (s - 2)) * 1024) : "memory");
        } else if (s == 13) {
          asm volatile("s_mov_b32 m0, %2\n\ts_nop 0\n\tglobal_load_lds_dwordx4 %0, %1 sc1" ::"v"((unsigned)lane * 16),
                       "s"(ws + OFF_ST + t0 * 8 + w * 1024), "s"(smb + G1_GOFF + w * 1024) : "memory");
        } else {
          float sq[8];
#pragma unroll
          for (int i = 0; i < 8; ++i) sq[i] = sv[i];
#pragma unroll
          for (int j = 0; j < 6; ++j)
            stat_add(sq, *(const u32x2*)(smem + G1_GOFF + (j * 32 + (lane & 31)) * 16 + (lane >> 5) * 8));
          float y[4];
#pragma unroll
          for (int i = 0; i < 4; ++i) {
            auto r = __builtin_amdgcn_permlane16_swap(__float_as_uint(sq[i]), __float_as_uint(sq[4 + i]), false, false);
            float z = __uint_as_float(r[0]) + __uint_as_float(r[1]);
            z += dpp<0x128>(z); z += dpp<0x124>(z); z += dpp<0x122>(z); z += dpp<0x121>(z);
            y[i] = z;
          }
          const int sel = lane & 3;
          const float z = sel == 0 ? y[0] : sel == 1 ? y[1] : sel == 2 ? y[2] : y[3];
          auto r = __builtin_amdgcn_permlane16_swap(__float_as_uint(z), __float_as_uint(z), false, false);
          const unsigned long long st = row_stat_of(__uint_as_float(r[0]), __uint_as_float(r[1]));
          if ((lane & 28) == 0)
            __hip_atomic_store((unsigned long long*)(ws + OFF_ST) + t0 + 8 * nt_ + 4 * (lane >> 5) + sel, st,
                               __ATOMIC_RELAXED, __HIP_MEMORY_SCOPE_AGENT);
        }
      }
      SCHED();
    }
  }
  int lane2 = threadIdx.x & 63;
  asm volatile("" : "+v"(lane2));
  const unsigned l16 = lane2 * 16;
  const int ksp = xg >> 1, nt2 = loc >> 3, k0 = ksp * 1152;
  const char* wbase = ws + OFF_W2T + ((size_t)(nt2 * 9) * WTS + (k0 >> 5)) * 1024;
  auto nof = [&](int a) { return a == 0 ? 8 : (2 * ksp + 1 + a) & 7; };
  unsigned vo1[3];
#pragma unroll
  for (int i = 0; i < 3; ++i) {
    int o = i * 1024 + l16;
    if (o >= 2304) o -= 768;
    const int p = w * 2304 + o;
    vo1[i] = nof(p >> 11) * (WTS * 1024) + (p & 2047);
  }
#define G2_GLDS1(i, j, slotoff)                                                                           \
  do { if ((i) < 2) glds16s(vo1[i], wbase + (j) * 2048, smb + (slotoff) + w * 2304 + (i) * 1024);           \
       else glds16s_q(vo1[i], wbase + (j) * 2048, smb + (slotoff) + w * 2304 + (i) * 1024); } while (0)
  int S3 = 3 * G1_STAGE;
  asm volatile("" : "+s"(S3));
  const int tr0 = t0 + 64 * tw + (lane2 & 31);
  g1_kstep(acc, Wa, Ha, Wb, Hb, hl0 + S3 + 16 * 512, hl1 + S3 + 16 * 512, wl + S3 + 16 * 288, five);
  if (!five) {
    const int ci = w * 64 + lane2;
    if (ci < 72) {
      const f32x4* cp = (const f32x4*)(smem + G1_CPOFF) + ci;
      ((f32x4*)(smem + G1_CSOFF))[ci] = ((cp[0] + cp[72]) + (cp[144] + cp[216])) + (cp[288] + cp[360]);
    }
  }
  WAIT_VL(0);
  BAR();
  if (five) __builtin_amdgcn_s_setprio(2);
  const unsigned long long* stl = (const unsigned long long*)(smem + G1_GOFF) + 64 * tw + (lane2 & 31);
  unsigned long long st0 = stl[0], st1 = stl[32];
  bf16x8 Ht[4][2], Wt[4];
  auto wrd = [&](int u) {
    const char LDSP* p = wl + ((u >> 1) & 1) * G1_STAGE + (u & 1) * (16 * 288) + (u >> 2) * 32;
    return cat8(tr_read(p), tr_read(p + 4 * 288));
  };
#pragma unroll
  for (int i = 0; i < 8; ++i) {
    acc[i >> 1][i & 1] = __builtin_amdgcn_mfma_f32_32x32x16_bf16(Wb[i >> 1], Hb[i & 1], acc[i >> 1][i & 1], 0, 0, 0);
    const char LDSP* p = ((i & 1) ? hl1 : hl0) + (i >> 2) * G1_STAGE + ((i >> 1) & 1) * (16 * 512);
    Ht[i >> 1][i & 1] = cat8(tr_read(p), tr_read(p + 4 * 512));
    if (i >= 5) Wt[i - 5] = wrd(i - 5);
    SCHED();
  }
  if (five) {
    acc[4][0] = __builtin_amdgcn_mfma_f32_32x32x16_bf16(Wb[4], Hb[0], acc[4][0], 0, 0, 0);
    acc[4][1] = __builtin_amdgcn_mfma_f32_32x32x16_bf16(Wb[4], Hb[1], acc[4][1], 0, 0, 0);
  }
  SCHED();
  {
    unsigned spins = 0;
    while (__any((unsigned)(st0 >> 32) == 0xffffffffu || (unsigned)(st1 >> 32) == 0xffffffffu)) {
      if (++spins > 20000u) {
        const bf16_t* hp = (const bf16_t*)(ws + OFF_HT) + tr0;
        float S0 = 0.f, Q0 = 0.f, S1_ = 0.f, Q1 = 0.f;
#pragma unroll 1
        for (int d = 0; d < D; ++d) {
          const float a = bf2f(hp[(size_t)d * L]), b = bf2f(hp[(size_t)d * L + 32]);
          S0 += a; Q0 += a * a; S1_ += b; Q1 += b * b;
        }
        st0 = row_stat_of(S0, Q0); st1 = row_stat_of(S1_, Q1);
        break;
      }
      __builtin_amdgcn_s_sleep(2);
      st0 = ld_stat(ws, tr0); st1 = ld_stat(ws, tr0 + 32);
    }
  }
  const int h = lane2 >> 5;
  const f32x4* csl = (const f32x4*)(smem + G1_CSOFF) + 4 * N0 + 2 * h;
  float nmu[2], sc[2], ir2[2];
#pragma unroll
  for (int mt = 0; mt < 2; ++mt) {
    const unsigned long long st = mt ? st1 : st0;
    const float rs = __uint_as_float((unsigned)(st >> 32));
    nmu[mt] = -__uint_as_float((unsigned)st);
    sc[mt] = -1.44269504088896341f * rs;
    ir2[mt] = __builtin_amdgcn_rcpf(rs * rs);
  }
  const unsigned hmrow = (unsigned)((tr0 >> 4) * HMS * 1024 + (tr0 & 15) * 16);
  const auto rhm = __builtin_amdgcn_make_buffer_rsrc(ws + OFF_HM, 0, 128 * HMS * 1024, 0x00020000);
  f32x4 cg, cu, ex, pr;
  u32x4 pk[2];
  auto epiA = [&](int n, int c_) {
    const int e0 = 4 * (c_ >> 1), mt = c_ & 1;
#pragma unroll
    for (int j = 0; j < 4; ++j) {
      const float g = acc[n][mt][e0 + j] + nmu[mt] * cg[j];
      const float u = acc[n][mt][8 + e0 + j] + nmu[mt] * cu[j];
      ex[j] = __builtin_amdgcn_exp2f(g * sc[mt]);
      pr[j] = g * u;
    }
    asm volatile("" : "+v"(ex), "+v"(pr));
    if (c_ == 1) { cg = csl[4 * n + 1]; cu = csl[4 * n + 37]; }
    if (c_ == 3 && n < 4) { cg = csl[4 * n + 4]; cu = csl[4 * n + 40]; }
  };
  auto epiB = [&](int n, int c_) {
    const int hf = c_ >> 1, mt = c_ & 1;
    float o[4];
#pragma unroll
    for (int j = 0; j < 4; ++j) o[j] = pr[j] * __builtin_amdgcn_rcpf(ex[j] * ir2[mt] + ir2[mt]);
    pk[mt][2 * hf] = pack_bf(o[0], o[1]);
    pk[mt][2 * hf + 1] = pack_bf(o[2], o[3]);
    asm volatile("" : "+v"(pk[mt]));
    if (hf == 1) {
      const int f = f0 + 16 * (N0 + n) + 8 * h;
      __builtin_amdgcn_raw_buffer_store_b128(
          pk[mt], rhm, hmrow + (f >> 5) * 1024 + ((f >> 3) & 3) * 256 + mt * (2 * HMS * 1024), 0, 16);
    }
  };
  auto tile = [&](int n) {
#pragma unroll
    for (int q = 0; q < 8; ++q) {
      const int u = 4 * n + (q >> 1), mt = q & 1;
      acc[n][mt] = __builtin_amdgcn_mfma_f32_32x32x16_bf16(Wt[u & 3], Ht[q >> 1][mt], acc[n][mt], 0, 0, 0);
      if (mt == 0 && u + 3 < 20) Wt[(u + 3) & 3] = wrd(u + 3);
      SCHED();
      if (n == 0) {
        if (q < 3) { G2_GLDS1(0, q, (4 + q) * G2_SLOT); G2_GLDS1(1, q, (4 + q) * G2_SLOT); G2_GLDS1(2, q, (4 + q) * G2_SLOT); }
      } else if (q & 1) epiB(n - 1, q >> 1);
      else epiA(n - 1, q >> 1);
      SCHED();
    }
  };
  auto epi = [&](int n) {
#pragma unroll
    for (int c_ = 0; c_ < 4; ++c_) { epiA(n, c_); epiB(n, c_); }
  };
  cg = csl[0]; cu = csl[36];
  tile(0); tile(1); tile(2); tile(3);
  if (five) {
    tile(4);
    epi(4);
  } else {
    epi(3);
  }
  __builtin_amdgcn_s_setprio(0);
  asm volatile("s_waitcnt vmcnt(0) lgkmcnt(0)" ::: "memory");
  BAR();
  if (threadIdx.x == 0)
    __hip_atomic_store((unsigned*)(ws + OFF_FL) + (mt_ * 32 + nt_) * 32, 1u, __ATOMIC_RELAXED, __HIP_MEMORY_SCOPE_AGENT);

  const int mt2 = 8 * (xg & 1) + (loc & 7);
  const int t2 = mt2 * 128, d0 = nt2 * 144;
  const int tile2 = mt2 * 4 + nt2;
  const int kh = w >> 2;
  const char* hb0 = ws + OFF_HM + ((size_t)(((t2 + 32 * tw) >> 4) + kh) * HMS + (k0 >> 5) + kh) * 1024;
  const char* hb1 = ws + OFF_HM + ((size_t)(((t2 + 32 * tw) >> 4) + 1 - kh) * HMS + (k0 >> 5) + kh) * 1024;
  constexpr int G2_B2 = (18 - G2_K1) * 256;
  const unsigned vo2 = w * G2_B2 + l16;
  const int kq = lane2 >> 4, i16b = lane2 & 15;
  const char LDSP* lb0 = (const char LDSP*)smem + kh * 1024 + l16;
  const char LDSP* lb1 = lb0 + 55296;
  const char LDSP* lb2 = lb0 + 110592;
  asm volatile("" : "+v"(lb1), "+v"(lb2));
  auto rs = __builtin_amdgcn_make_buffer_rsrc(ws + OFF_SL, 0, 64 * 4 * 8 * 4096, 0x00020000);
  const unsigned slab_w = (unsigned)((tile2 * 4 + ksp) * 8 + w) * 4096u;
  const int trow = t2 + 32 * tw + 16 * kh + i16b;
  const char* hres_p = ws + OFF_HT + ((size_t)(d0 + 4 * kq) * L + trow) * 2;

  bf16x8 H[18][2], W[10];
  f32x4 ac2[9][2];
  unsigned res[12];
  const f32x4 zero4 = {0.f, 0.f, 0.f, 0.f};
#define G2_GLDS2(i, a, slotoff)                                                                              \
  do {                                                                                                       \
    const char* src_ = wbase + nof(a) * (WTS * 1024) + 2 * G2_K1 * 1024 + (i) * 1024;                        \
    const unsigned dst_ = smb + (slotoff) + w * G2_B2 + (i) * 1024;                                          \
    if (((i) + 1) * 1024 <= G2_B2) glds16s(vo2, src_, dst_);                                                 \
    else if (G2_B2 - (i) * 1024 == 512) glds16s_h(vo2, src_, dst_);                                          \
    else glds16s_q(vo2, src_, dst_);                                                                         \
  } while (0)
#define G2_HLD(hs, tt)                                                    \
  asm volatile("global_load_dwordx4 %0, %1, %2 sc1"                       \
               : "=v"(H[hs][tt]) : "v"(l16), "s"(hb##tt + (hs) * 2048) : "memory")
#define G2_PINH(hs) asm volatile("" : "+v"(H[hs][0]), "+v"(H[hs][1]))
#define G2_RD(dst, base, off) dst = *(const bf16x8 LDSP*)(base + (off))
#define G2_MF(a, tt, Wf, hs, first) \
  ac2[a][tt] = __builtin_amdgcn_mfma_f32_16x16x32_bf16(Wf, H[hs][tt], (first) ? zero4 : ac2[a][tt], 0, 0, 0)
#define G2_XB(a) (G2_XOFF + 8192 * ((a) == 0 || (a) == 3 || (a) == 7 ? 0 : (a) == 1 || (a) == 5 ? 1 : (a) == 2 || (a) == 6 ? 2 : 3))
#define G2_XWRITE(a) *(f32x4*)(smem + G2_XB(a) + w * 1024 + l16) = ac2[a][1]
#define G2_XRD(a) ac2[a][1] = *(const f32x4*)(smem + G2_XB(a) + (w ^ 4) * 1024 + l16)
#define G2_XADD(a) ac2[a][0] += ac2[a][1]
#define G2_XREAD(a) do { G2_XRD(a); G2_XADD(a); } while (0)
#define G2_SLAB2(a)                                                                                              \
  __builtin_amdgcn_raw_buffer_store_b128(                                                                        \
      not_sentinel(u32x4{pack_bf(ac2[(a) - 1][0][0], ac2[(a) - 1][0][1]), pack_bf(ac2[(a) - 1][0][2], ac2[(a) - 1][0][3]), \
                         pack_bf(ac2[a][0][0], ac2[a][0][1]), pack_bf(ac2[a][0][2], ac2[a][0][3])}),             \
      rs, slab_w + ((a) / 2 - 1) * 1024 + l16, 0, 16)
#define G2_SLAB0()                                                                                               \
  do {                                                                                                           \
    const u32x4 v_ = not_sentinel(u32x4{pack_bf(ac2[0][0][0], ac2[0][0][1]), pack_bf(ac2[0][0][2], ac2[0][0][3]), 0, 0}); \
    __builtin_amdgcn_raw_buffer_store_b64(u32x2{v_[0], v_[1]}, rs, slab_w + 3072 + lane2 * 8, 0, 16);             \
  } while (0)
  u32x4 part[3];
  u32x2 parts[3];
  auto pload = [&](int o) {
    const unsigned off = (unsigned)((tile2 * 4 + ((ksp + 1 + o) & 3)) * 8 + w) * 4096u + (2 - o) * 1024 + l16;
    return __builtin_amdgcn_raw_buffer_load_b128(rs, off, 0, 16);
  };
  auto ploads = [&](int o) {
    const unsigned off = (unsigned)((tile2 * 4 + ((ksp + 1 + o) & 3)) * 8 + w) * 4096u + 3072 + lane2 * 8;
    return __builtin_amdgcn_raw_buffer_load_b64(rs, off, 0, 16);
  };
#define G2_PLOAD(o) part[o] = pload(o)
#define G2_PLOADS() do { parts[0] = ploads(0); parts[1] = ploads(1); parts[2] = ploads(2); } while (0)
#define G2_RESP(e) (hres_p + ((size_t)(16 * ((e) < 8 ? nof(7 + ((e) >> 2)) : 8) + ((e) & 3)) * L) * 2)
#define G2_RES(e) asm volatile("global_load_ushort %0, %1, off" : "=v"(res[e]) : "v"(G2_RESP(e)) : "memory")
  const unsigned* rdy = (const unsigned*)(ws + OFF_FL);
  const unsigned* rp0 = rdy + ((mt2 >> 1) * 32 + 8 * ksp + (lane2 & 7)) * 32;
  const int pxg = (xg & 1) + 2 * ((ksp + 1 + (lane2 & 3)) & 3);
  const unsigned* rp2 = rdy + ((4 * (pxg & 1) + (loc & 3)) * 32 + 8 * (pxg >> 1) + (loc >> 2)) * 32;
  unsigned pv;
  auto spin = [&](const unsigned* p) {
    unsigned spins = 0;
    while (__any(pv == 0) && ++spins < (1u << 17)) {
      __builtin_amdgcn_s_sleep(1);
      pv = __hip_atomic_load(p, __ATOMIC_RELAXED, __HIP_MEMORY_SCOPE_AGENT);
    }
  };
#define G2_POLL0() do { pv = 0; if (w == 0) spin(rp0); } while (0)
#define G2_POLL1(n) do { BAR(); } while (0)
#define G2_PQ0() asm volatile("global_load_dword %0, %1, off sc1" : "=v"(pv) : "v"(rp2) : "memory")
#define G2_POLL2() do { asm volatile("" : "+v"(pv)); spin(rp2); } while (0)
  G2_POLL0();
  G2_GLDS1(0, 3, 0);
  G2_GLDS1(1, 3, 0);
  G2_GLDS1(2, 3, 0);
  G2_GLDS1(0, 4, 18432);
  G2_GLDS1(1, 4, 18432);
  G2_GLDS1(2, 4, 18432);
  G2_GLDS1(0, 5, 36864);
  G2_GLDS1(1, 5, 36864);
  G2_GLDS1(2, 5, 36864);
  G2_GLDS1(0, 6, 55296);
  G2_GLDS1(1, 6, 55296);
  G2_GLDS1(2, 6, 55296);
  G2_POLL1(12);
  G2_HLD(0, 0);
  G2_HLD(0, 1);
  G2_HLD(9, 0);
  G2_HLD(9, 1);
  G2_HLD(1, 0);
  G2_HLD(1, 1);
  G2_HLD(10, 0);
  G2_HLD(10, 1);
  G2_HLD(2, 0);
  G2_HLD(2, 1);
  G2_HLD(11, 0);
  G2_HLD(11, 1);
  WAIT_VL(10);
  G2_PINH(0);
  BAR();
  G2_RD(W[0], lb1, 18432);
  G2_RD(W[1], lb1, 20480);
  G2_RD(W[2], lb1, 22528);
  G2_RD(W[3], lb1, 24576);
  G2_RD(W[4], lb1, 26624);
  G2_RD(W[5], lb1, 28672);
  SCHED();
  G2_MF(0, 0, W[0], 0, 1);
  G2_RD(W[6], lb1, 30720);
  SCHED();
  G2_MF(0, 1, W[0], 0, 1);
  G2_HLD(3, 0);
  SCHED();
  G2_MF(1, 0, W[1], 0, 1);
  G2_RD(W[7], lb1, 32768);
  SCHED();
  G2_MF(1, 1, W[1], 0, 1);
  G2_HLD(3, 1);
  SCHED();
  G2_MF(2, 0, W[2], 0, 1);
  G2_RD(W[8], lb1, 34816);
  SCHED();
  G2_MF(2, 1, W[2], 0, 1);
  G2_HLD(12, 0);
  SCHED();
  WAIT_VL(9);
  G2_PINH(1);
  BAR();
  G2_MF(3, 0, W[3], 0, 1);
  G2_RD(W[9], lb1, 36864);
  SCHED();
  G2_MF(3, 1, W[3], 0, 1);
  G2_HLD(12, 1);
  SCHED();
  G2_MF(4, 0, W[4], 0, 1);
  G2_RD(W[0], lb1, 38912);
  SCHED();
  G2_MF(4, 1, W[4], 0, 1);
  G2_HLD(4, 0);
  SCHED();
  G2_MF(5, 0, W[5], 0, 1);
  G2_RD(W[1], lb1, 40960);
  SCHED();
  G2_MF(5, 1, W[5], 0, 1);
  G2_HLD(4, 1);
  SCHED();
  G2_MF(6, 0, W[6], 0, 1);
  G2_RD(W[2], lb1, 43008);
  SCHED();
  G2_MF(6, 1, W[6], 0, 1);
  G2_HLD(13, 0);
  SCHED();
  G2_MF(7, 0, W[7], 0, 1);
  G2_RD(W[3], lb1, 45056);
  SCHED();
  G2_MF(7, 1, W[7], 0, 1);
  G2_HLD(13, 1);
  SCHED();
  G2_MF(8, 0, W[8], 0, 1);
  G2_RD(W[4], lb1, 47104);
  SCHED();
  G2_MF(8, 1, W[8], 0, 1);
  SCHED();
  G2_MF(0, 0, W[9], 1, 0);
  G2_RD(W[5], lb1, 49152);
  SCHED();
  G2_MF(0, 1, W[9], 1, 0);
  G2_HLD(5, 0);
  SCHED();
  G2_MF(1, 0, W[0], 1, 0);
  G2_RD(W[6], lb1, 51200);
  SCHED();
  G2_MF(1, 1, W[0], 1, 0);
  G2_HLD(5, 1);
  SCHED();
  G2_MF(2, 0, W[1], 1, 0);
  G2_RD(W[7], lb1, 53248);
  SCHED();
  G2_MF(2, 1, W[1], 1, 0);
  G2_HLD(14, 0);
  SCHED();
  WAIT_VL(13);
  G2_PINH(2);
  BAR();
  G2_MF(3, 0, W[2], 1, 0);
  G2_RD(W[8], lb2, 0);
  SCHED();
  G2_MF(3, 1, W[2], 1, 0);
  G2_HLD(14, 1);
  SCHED();
  G2_MF(4, 0, W[3], 1, 0);
  G2_RD(W[9], lb2, 2048);
  SCHED();
  G2_MF(4, 1, W[3], 1, 0);
  SCHED();
  G2_MF(5, 0, W[4], 1, 0);
  G2_RD(W[0], lb2, 4096);
  SCHED();
  G2_MF(5, 1, W[4], 1, 0);
  SCHED();
  G2_MF(6, 0, W[5], 1, 0);
  G2_RD(W[1], lb2, 6144);
  SCHED();
  G2_MF(6, 1, W[5], 1, 0);
  SCHED();
  G2_MF(7, 0, W[6], 1, 0);
  G2_RD(W[2], lb2, 8192);
  SCHED();
  G2_MF(7, 1, W[6], 1, 0);
  SCHED();
  G2_MF(8, 0, W[7], 1, 0);
  G2_RD(W[3], lb2, 10240);
  SCHED();
  G2_MF(8, 1, W[7], 1, 0);
  SCHED();
  G2_MF(0, 0, W[8], 2, 0);
  G2_RD(W[4], lb2, 12288);
  SCHED();
  G2_MF(0, 1, W[8], 2, 0);
  G2_HLD(6, 0);
  SCHED();
  G2_MF(1, 0, W[9], 2, 0);
  G2_RD(W[5], lb2, 14336);
  SCHED();
  G2_MF(1, 1, W[9], 2, 0);
  G2_HLD(6, 1);
  SCHED();
  G2_MF(2, 0, W[0], 2, 0);
  G2_RD(W[6], lb2, 16384);
  SCHED();
  G2_MF(2, 1, W[0], 2, 0);
  G2_HLD(15, 0);
  SCHED();
  WAIT_VL(13);
  G2_PINH(3);
  BAR();
  G2_MF(3, 0, W[1], 2, 0);
  G2_RD(W[7], lb0, 0);
  SCHED();
  G2_MF(3, 1, W[1], 2, 0);
  G2_HLD(15, 1);
  SCHED();
  G2_MF(4, 0, W[2], 2, 0);
  G2_RD(W[8], lb0, 2048);
  SCHED();
  G2_MF(4, 1, W[2], 2, 0);
  G2_GLDS1(0, 7, 73728);
  SCHED();
  G2_MF(5, 0, W[3], 2, 0);
  G2_RD(W[9], lb0, 4096);
  SCHED();
  G2_MF(5, 1, W[3], 2, 0);
  G2_GLDS1(1, 7, 73728);
  SCHED();
  G2_MF(6, 0, W[4], 2, 0);
  G2_RD(W[0], lb0, 6144);
  SCHED();
  G2_MF(6, 1, W[4], 2, 0);
  G2_GLDS1(2, 7, 73728);
  SCHED();
  G2_MF(7, 0, W[5], 2, 0);
  G2_RD(W[1], lb0, 8192);
  SCHED();
  G2_MF(7, 1, W[5], 2, 0);
  SCHED();
  G2_MF(8, 0, W[6], 2, 0);
  G2_RD(W[2], lb0, 10240);
  SCHED();
  G2_MF(8, 1, W[6], 2, 0);
  SCHED();
  G2_MF(0, 0, W[7], 3, 0);
  G2_RD(W[3], lb0, 12288);
  SCHED();
  G2_MF(0, 1, W[7], 3, 0);
  G2_HLD(7, 0);
  SCHED();
  G2_MF(1, 0, W[8], 3, 0);
  G2_RD(W[4], lb0, 14336);
  SCHED();
  G2_MF(1, 1, W[8], 3, 0);
  G2_HLD(7, 1);
  SCHED();
  G2_MF(2, 0, W[9], 3, 0);
  G2_RD(W[5], lb0, 16384);
  SCHED();
  G2_MF(2, 1, W[9], 3, 0);
  G2_HLD(16, 0);
  SCHED();
  WAIT_VL(16);
  G2_PINH(4);
  BAR();
  G2_MF(3, 0, W[0], 3, 0);
  G2_RD(W[6], lb0, 18432);
  SCHED();
  G2_MF(3, 1, W[0], 3, 0);
  G2_HLD(16, 1);
  SCHED();
  G2_MF(4, 0, W[1], 3, 0);
  G2_RD(W[7], lb0, 20480);
  SCHED();
  G2_MF(4, 1, W[1], 3, 0);
  G2_GLDS1(0, 8, 92160);
  SCHED();
  G2_MF(5, 0, W[2], 3, 0);
  G2_RD(W[8], lb0, 22528);
  SCHED();
  G2_MF(5, 1, W[2], 3, 0);
  G2_GLDS1(1, 8, 92160);
  SCHED();
  G2_MF(6, 0, W[3], 3, 0);
  G2_RD(W[9], lb0, 24576);
  SCHED();
  G2_MF(6, 1, W[3], 3, 0);
  G2_GLDS1(2, 8, 92160);
  SCHED();
  G2_MF(7, 0, W[4], 3, 0);
  G2_RD(W[0], lb0, 26624);
  SCHED();
  G2_MF(7, 1, W[4], 3, 0);
  SCHED();
  G2_MF(8, 0, W[5], 3, 0);
  G2_RD(W[1], lb0, 28672);
  SCHED();
  G2_MF(8, 1, W[5], 3, 0);
  SCHED();
  G2_MF(0, 0, W[6], 4, 0);
  G2_RD(W[2], lb0, 30720);
  SCHED();
  G2_MF(0, 1, W[6], 4, 0);
  G2_HLD(8, 0);
  SCHED();
  G2_MF(1, 0, W[7], 4, 0);
  G2_RD(W[3], lb0, 32768);
  SCHED();
  G2_MF(1, 1, W[7], 4, 0);
  G2_HLD(8, 1);
  SCHED();
  G2_MF(2, 0, W[8], 4, 0);
  G2_RD(W[4], lb0, 34816);
  SCHED();
  G2_MF(2, 1, W[8], 4, 0);
  G2_HLD(17, 0);
  SCHED();
  WAIT_VL(19);
  G2_PINH(5);
  BAR();
  G2_MF(3, 0, W[9], 4, 0);
  G2_RD(W[5], lb0, 36864);
  SCHED();
  G2_MF(3, 1, W[9], 4, 0);
  G2_HLD(17, 1);
  SCHED();
  G2_MF(4, 0, W[0], 4, 0);
  G2_RD(W[6], lb0, 38912);
  SCHED();
  G2_MF(4, 1, W[0], 4, 0);
  G2_GLDS2(0, 0, 110592);
  SCHED();
  G2_MF(5, 0, W[1], 4, 0);
  G2_RD(W[7], lb0, 40960);
  SCHED();
  G2_MF(5, 1, W[1], 4, 0);
  G2_GLDS2(1, 0, 110592);
  SCHED();
  G2_MF(6, 0, W[2], 4, 0);
  G2_RD(W[8], lb0, 43008);
  SCHED();
  G2_MF(6, 1, W[2], 4, 0);
  G2_GLDS2(2, 0, 110592);
  SCHED();
  G2_MF(7, 0, W[3], 4, 0);
  G2_RD(W[9], lb0, 45056);
  SCHED();
  G2_MF(7, 1, W[3], 4, 0);
  G2_GLDS2(0, 1, 0);
  SCHED();
  G2_MF(8, 0, W[4], 4, 0);
  G2_RD(W[0], lb0, 47104);
  SCHED();
  G2_MF(8, 1, W[4], 4, 0);
  G2_GLDS2(1, 1, 0);
  SCHED();
  G2_MF(0, 0, W[5], 5, 0);
  G2_RD(W[1], lb0, 49152);
  SCHED();
  G2_MF(0, 1, W[5], 5, 0);
  G2_GLDS2(2, 1, 0);
  SCHED();
  G2_MF(1, 0, W[6], 5, 0);
  G2_RD(W[2], lb0, 51200);
  SCHED();
  G2_MF(1, 1, W[6], 5, 0);
  G2_GLDS2(0, 2, 18432);
  SCHED();
  G2_MF(2, 0, W[7], 5, 0);
  G2_RD(W[3], lb0, 53248);
  SCHED();
  G2_MF(2, 1, W[7], 5, 0);
  G2_GLDS2(1, 2, 18432);
  SCHED();
  WAIT_VL(24);
  G2_PINH(6);
  BAR();
  G2_MF(3, 0, W[8], 5, 0);
  G2_RD(W[4], lb1, 0);
  SCHED();
  G2_MF(3, 1, W[8], 5, 0);
  G2_GLDS2(2, 2, 18432);
  SCHED();
  G2_MF(4, 0, W[9], 5, 0);
  G2_RD(W[5], lb1, 2048);
  SCHED();
  G2_MF(4, 1, W[9], 5, 0);
  G2_GLDS2(0, 3, 36864);
  SCHED();
  G2_MF(5, 0, W[0], 5, 0);
  G2_RD(W[6], lb1, 4096);
  SCHED();
  G2_MF(5, 1, W[0], 5, 0);
  G2_GLDS2(1, 3, 36864);
  SCHED();
  G2_MF(6, 0, W[1], 5, 0);
  G2_RD(W[7], lb1, 6144);
  SCHED();
  G2_MF(6, 1, W[1], 5, 0);
  G2_GLDS2(2, 3, 36864);
  SCHED();
  G2_MF(7, 0, W[2], 5, 0);
  G2_RD(W[8], lb1, 8192);
  SCHED();
  G2_MF(7, 1, W[2], 5, 0);
  SCHED();
  G2_MF(8, 0, W[3], 5, 0);
  G2_RD(W[9], lb1, 10240);
  SCHED();
  G2_MF(8, 1, W[3], 5, 0);
  SCHED();
  G2_MF(0, 0, W[4], 6, 0);
  G2_RD(W[0], lb1, 12288);
  SCHED();
  G2_MF(0, 1, W[4], 6, 0);
  SCHED();
  G2_MF(1, 0, W[5], 6, 0);
  G2_RD(W[1], lb1, 14336);
  SCHED();
  G2_MF(1, 1, W[5], 6, 0);
  SCHED();
  G2_MF(2, 0, W[6], 6, 0);
  G2_RD(W[2], lb1, 16384);
  SCHED();
  G2_MF(2, 1, W[6], 6, 0);
  SCHED();
  WAIT_VL(21);
  G2_PINH(7);
  BAR();
  G2_MF(3, 0, W[7], 6, 0);
  G2_RD(W[3], lb1, 18432);
  SCHED();
  G2_MF(3, 1, W[7], 6, 0);
  G2_GLDS2(0, 4, 55296);
  SCHED();
  G2_MF(4, 0, W[8], 6, 0);
  G2_RD(W[4], lb1, 20480);
  SCHED();
  G2_MF(4, 1, W[8], 6, 0);
  G2_GLDS2(1, 4, 55296);
  SCHED();
  G2_MF(5, 0, W[9], 6, 0);
  G2_RD(W[5], lb1, 22528);
  SCHED();
  G2_MF(5, 1, W[9], 6, 0);
  G2_GLDS2(2, 4, 55296);
  SCHED();
  G2_MF(6, 0, W[0], 6, 0);
  G2_RD(W[6], lb1, 24576);
  SCHED();
  G2_MF(6, 1, W[0], 6, 0);
  SCHED();
  G2_MF(7, 0, W[1], 6, 0);
  G2_RD(W[7], lb1, 26624);
  SCHED();
  G2_MF(7, 1, W[1], 6, 0);
  SCHED();
  G2_MF(8, 0, W[2], 6, 0);
  G2_RD(W[8], lb1, 28672);
  SCHED();
  G2_MF(8, 1, W[2], 6, 0);
  SCHED();
  G2_MF(0, 0, W[3], 7, 0);
  G2_RD(W[9], lb1, 30720);
  SCHED();
  G2_MF(0, 1, W[3], 7, 0);
  SCHED();
  G2_MF(1, 0, W[4], 7, 0);
  G2_RD(W[0], lb1, 32768);
  SCHED();
  G2_MF(1, 1, W[4], 7, 0);
  SCHED();
  G2_MF(2, 0, W[5], 7, 0);
  G2_RD(W[1], lb1, 34816);
  SCHED();
  G2_MF(2, 1, W[5], 7, 0);
  SCHED();
  WAIT_VL(17);
  G2_PINH(8);
  BAR();
  G2_MF(3, 0, W[6], 7, 0);
  G2_RD(W[2], lb1, 36864);
  SCHED();
  G2_MF(3, 1, W[6], 7, 0);
  G2_GLDS2(0, 5, 73728);
  SCHED();
  G2_MF(4, 0, W[7], 7, 0);
  G2_RD(W[3], lb1, 38912);
  SCHED();
  G2_MF(4, 1, W[7], 7, 0);
  G2_GLDS2(1, 5, 73728);
  SCHED();
  G2_MF(5, 0, W[8], 7, 0);
  G2_RD(W[4], lb1, 40960);
  SCHED();
  G2_MF(5, 1, W[8], 7, 0);
  G2_GLDS2(2, 5, 73728);
  SCHED();
  G2_MF(6, 0, W[9], 7, 0);
  G2_RD(W[5], lb1, 43008);
  SCHED();
  G2_MF(6, 1, W[9], 7, 0);
  SCHED();
  G2_MF(7, 0, W[0], 7, 0);
  G2_RD(W[6], lb1, 45056);
  SCHED();
  G2_MF(7, 1, W[0], 7, 0);
  SCHED();
  G2_MF(8, 0, W[1], 7, 0);
  G2_RD(W[7], lb1, 47104);
  SCHED();
  G2_MF(8, 1, W[1], 7, 0);
  SCHED();
  G2_MF(0, 0, W[2], 8, 0);
  G2_RD(W[8], lb1, 49152);
  SCHED();
  G2_MF(0, 1, W[2], 8, 0);
  SCHED();
  G2_MF(1, 0, W[3], 8, 0);
  G2_RD(W[9], lb1, 51200);
  SCHED();
  G2_MF(1, 1, W[3], 8, 0);
  SCHED();
  G2_RD(W[0], lb1, 53248);
  WAIT_VL(12);
  G2_PINH(9);
  G2_PINH(10);
  G2_PINH(11);
  G2_PINH(12);
  G2_PINH(13);
  G2_PINH(14);
  G2_PINH(15);
  G2_PINH(16);
  G2_PINH(17);
  BAR();
  G2_MF(2, 0, W[4], 8, 0);
  G2_RD(W[1], lb2, 0);
  SCHED();
  G2_MF(2, 1, W[4], 8, 0);
  G2_GLDS2(0, 6, 92160);
  SCHED();
  G2_MF(3, 0, W[5], 8, 0);
  G2_RD(W[2], lb2, 2048);
  SCHED();
  G2_MF(3, 1, W[5], 8, 0);
  G2_GLDS2(1, 6, 92160);
  SCHED();
  G2_MF(4, 0, W[6], 8, 0);
  G2_RD(W[3], lb2, 4096);
  SCHED();
  G2_MF(4, 1, W[6], 8, 0);
  G2_GLDS2(2, 6, 92160);
  SCHED();
  G2_MF(5, 0, W[7], 8, 0);
  G2_RD(W[4], lb2, 6144);
  SCHED();
  G2_MF(5, 1, W[7], 8, 0);
  SCHED();
  G2_MF(6, 0, W[8], 8, 0);
  G2_RD(W[5], lb2, 8192);
  SCHED();
  G2_MF(6, 1, W[8], 8, 0);
  SCHED();
  G2_MF(7, 0, W[9], 8, 0);
  G2_RD(W[6], lb2, 10240);
  SCHED();
  G2_MF(7, 1, W[9], 8, 0);
  SCHED();
  G2_MF(8, 0, W[0], 8, 0);
  G2_RD(W[7], lb2, 12288);
  SCHED();
  G2_MF(8, 1, W[0], 8, 0);
  SCHED();
  G2_MF(0, 0, W[1], 9, 0);
  G2_RD(W[8], lb2, 14336);
  SCHED();
  G2_MF(0, 1, W[1], 9, 0);
  SCHED();
  G2_MF(0, 0, W[2], 10, 0);
  G2_RD(W[9], lb2, 16384);
  SCHED();
  G2_MF(0, 1, W[2], 10, 0);
  SCHED();
  G2_MF(0, 0, W[3], 11, 0);
  G2_RD(W[0], lb0, 0);
  SCHED();
  G2_MF(0, 1, W[3], 11, 0);
  SCHED();
  G2_MF(0, 0, W[4], 12, 0);
  G2_RD(W[1], lb0, 2048);
  SCHED();
  G2_MF(0, 1, W[4], 12, 0);
  SCHED();
  G2_MF(0, 0, W[5], 13, 0);
  G2_RD(W[2], lb0, 4096);
  SCHED();
  G2_MF(0, 1, W[5], 13, 0);
  SCHED();
  G2_MF(0, 0, W[6], 14, 0);
  G2_RD(W[3], lb0, 6144);
  SCHED();
  G2_MF(0, 1, W[6], 14, 0);
  SCHED();
  G2_MF(0, 0, W[7], 15, 0);
  G2_RD(W[4], lb0, 8192);
  SCHED();
  G2_MF(0, 1, W[7], 15, 0);
  SCHED();
  G2_MF(0, 0, W[8], 16, 0);
  G2_RD(W[5], lb0, 10240);
  SCHED();
  G2_MF(0, 1, W[8], 16, 0);
  SCHED();
  G2_MF(0, 0, W[9], 17, 0);
  G2_RD(W[6], lb0, 12288);
  SCHED();
  G2_MF(0, 1, W[9], 17, 0);
  SCHED();
  G2_PQ0(); SCHED();
  G2_MF(1, 0, W[0], 9, 0);
  G2_RD(W[7], lb0, 14336);
  SCHED();
  G2_MF(1, 1, W[0], 9, 0);
  SCHED();
  G2_MF(1, 0, W[1], 10, 0);
  G2_RD(W[8], lb0, 16384);
  SCHED();
  G2_MF(1, 1, W[1], 10, 0);
  SCHED();
  G2_XWRITE(0);
  SCHED();
  WAIT_VL(10);
  BAR();
  G2_XRD(0);
  G2_MF(1, 0, W[2], 11, 0);
  G2_RD(W[9], lb0, 18432);
  SCHED();
  G2_MF(1, 1, W[2], 11, 0);
  G2_GLDS2(0, 7, 110592);
  SCHED();
  G2_MF(1, 0, W[3], 12, 0);
  G2_RD(W[0], lb0, 20480);
  SCHED();
  G2_MF(1, 1, W[3], 12, 0);
  G2_GLDS2(1, 7, 110592);
  SCHED();
  G2_MF(1, 0, W[4], 13, 0);
  G2_RD(W[1], lb0, 22528);
  SCHED();
  G2_MF(1, 1, W[4], 13, 0);
  G2_GLDS2(2, 7, 110592);
  SCHED();
  G2_MF(1, 0, W[5], 14, 0);
  G2_RD(W[2], lb0, 24576);
  SCHED();
  G2_MF(1, 1, W[5], 14, 0);
  G2_XADD(0);
  G2_SLAB0();
  SCHED();
  G2_MF(1, 0, W[6], 15, 0);
  G2_RD(W[3], lb0, 26624);
  SCHED();
  G2_MF(1, 1, W[6], 15, 0);
  G2_GLDS2(0, 8, 0);
  SCHED();
  G2_MF(1, 0, W[7], 16, 0);
  G2_RD(W[4], lb0, 28672);
  SCHED();
  G2_MF(1, 1, W[7], 16, 0);
  G2_GLDS2(1, 8, 0);
  SCHED();
  G2_MF(1, 0, W[8], 17, 0);
  G2_RD(W[5], lb0, 30720);
  SCHED();
  G2_MF(1, 1, W[8], 17, 0);
  G2_GLDS2(2, 8, 0);
  SCHED();
  G2_MF(2, 0, W[9], 9, 0);
  G2_RD(W[6], lb0, 32768);
  SCHED();
  G2_MF(2, 1, W[9], 9, 0);
  G2_RES(0);
  SCHED();
  G2_MF(2, 0, W[0], 10, 0);
  G2_RD(W[7], lb0, 34816);
  SCHED();
  G2_MF(2, 1, W[0], 10, 0);
  G2_RES(1);
  SCHED();
  G2_XWRITE(1);
  SCHED();
  G2_MF(2, 0, W[1], 11, 0);
  G2_RD(W[8], lb0, 36864);
  SCHED();
  G2_MF(2, 1, W[1], 11, 0);
  G2_RES(2);
  SCHED();
  G2_MF(2, 0, W[2], 12, 0);
  G2_RD(W[9], lb0, 38912);
  SCHED();
  G2_MF(2, 1, W[2], 12, 0);
  G2_RES(3);
  SCHED();
  G2_MF(2, 0, W[3], 13, 0);
  G2_RD(W[0], lb0, 40960);
  SCHED();
  G2_MF(2, 1, W[3], 13, 0);
  G2_RES(4);
  SCHED();
  G2_MF(2, 0, W[4], 14, 0);
  G2_RD(W[1], lb0, 43008);
  SCHED();
  G2_MF(2, 1, W[4], 14, 0);
  G2_RES(5);
  SCHED();
  G2_MF(2, 0, W[5], 15, 0);
  G2_RD(W[2], lb0, 45056);
  SCHED();
  G2_MF(2, 1, W[5], 15, 0);
  G2_RES(6);
  SCHED();
  G2_MF(2, 0, W[6], 16, 0);
  G2_RD(W[3], lb0, 47104);
  SCHED();
  G2_MF(2, 1, W[6], 16, 0);
  G2_RES(7);
  SCHED();
  G2_MF(2, 0, W[7], 17, 0);
  G2_RD(W[4], lb0, 49152);
  SCHED();
  G2_MF(2, 1, W[7], 17, 0);
  G2_RES(8);
  SCHED();
  G2_MF(3, 0, W[8], 9, 0);
  G2_RD(W[5], lb0, 51200);
  SCHED();
  G2_MF(3, 1, W[8], 9, 0);
  G2_RES(9);
  SCHED();
  G2_MF(3, 0, W[9], 10, 0);
  G2_RD(W[6], lb0, 53248);
  SCHED();
  G2_MF(3, 1, W[9], 10, 0);
  G2_RES(10);
  SCHED();
  G2_XWRITE(2);
  SCHED();
  WAIT_VL(22);
  BAR();
  G2_XRD(1);
  G2_XRD(2);
  G2_MF(3, 0, W[0], 11, 0);
  G2_RD(W[7], lb1, 0);
  SCHED();
  G2_MF(3, 1, W[0], 11, 0);
  G2_RES(11);
  SCHED();
  G2_MF(3, 0, W[1], 12, 0);
  G2_RD(W[8], lb1, 2048);
  SCHED();
  G2_MF(3, 1, W[1], 12, 0);
  SCHED();
  G2_MF(3, 0, W[2], 13, 0);
  G2_RD(W[9], lb1, 4096);
  SCHED();
  G2_MF(3, 1, W[2], 13, 0);
  SCHED();
  G2_MF(3, 0, W[3], 14, 0);
  G2_RD(W[0], lb1, 6144);
  SCHED();
  G2_MF(3, 1, W[3], 14, 0);
  G2_XADD(1);
  G2_XADD(2);
  G2_SLAB2(2);
  SCHED();
  G2_MF(3, 0, W[4], 15, 0);
  G2_RD(W[1], lb1, 8192);
  SCHED();
  G2_MF(3, 1, W[4], 15, 0);
  SCHED();
  G2_MF(3, 0, W[5], 16, 0);
  G2_RD(W[2], lb1, 10240);
  SCHED();
  G2_MF(3, 1, W[5], 16, 0);
  SCHED();
  G2_MF(3, 0, W[6], 17, 0);
  G2_RD(W[3], lb1, 12288);
  SCHED();
  G2_MF(3, 1, W[6], 17, 0);
  SCHED();
  G2_MF(4, 0, W[7], 9, 0);
  G2_RD(W[4], lb1, 14336);
  SCHED();
  G2_MF(4, 1, W[7], 9, 0);
  SCHED();
  G2_MF(4, 0, W[8], 10, 0);
  G2_RD(W[5], lb1, 16384);
  SCHED();
  G2_MF(4, 1, W[8], 10, 0);
  SCHED();
  G2_XWRITE(3);
  SCHED();
  G2_MF(4, 0, W[9], 11, 0);
  G2_RD(W[6], lb1, 18432);
  SCHED();
  G2_MF(4, 1, W[9], 11, 0);
  SCHED();
  G2_MF(4, 0, W[0], 12, 0);
  G2_RD(W[7], lb1, 20480);
  SCHED();
  G2_MF(4, 1, W[0], 12, 0);
  SCHED();
  G2_MF(4, 0, W[1], 13, 0);
  G2_RD(W[8], lb1, 22528);
  SCHED();
  G2_MF(4, 1, W[1], 13, 0);
  SCHED();
  G2_MF(4, 0, W[2], 14, 0);
  G2_RD(W[9], lb1, 24576);
  SCHED();
  G2_MF(4, 1, W[2], 14, 0);
  SCHED();
  G2_MF(4, 0, W[3], 15, 0);
  G2_RD(W[0], lb1, 26624);
  SCHED();
  G2_MF(4, 1, W[3], 15, 0);
  SCHED();
  G2_MF(4, 0, W[4], 16, 0);
  G2_RD(W[1], lb1, 28672);
  SCHED();
  G2_MF(4, 1, W[4], 16, 0);
  SCHED();
  G2_MF(4, 0, W[5], 17, 0);
  G2_RD(W[2], lb1, 30720);
  SCHED();
  G2_MF(4, 1, W[5], 17, 0);
  SCHED();
  G2_MF(5, 0, W[6], 9, 0);
  G2_RD(W[3], lb1, 32768);
  SCHED();
  G2_MF(5, 1, W[6], 9, 0);
  SCHED();
  G2_MF(5, 0, W[7], 10, 0);
  G2_RD(W[4], lb1, 34816);
  SCHED();
  G2_MF(5, 1, W[7], 10, 0);
  SCHED();
  G2_XWRITE(4);
  SCHED();
  WAIT_VL(17);
  BAR();
  G2_XRD(3);
  G2_XRD(4);
  G2_MF(5, 0, W[8], 11, 0);
  G2_RD(W[5], lb1, 36864);
  SCHED();
  G2_MF(5, 1, W[8], 11, 0);
  SCHED();
  G2_MF(5, 0, W[9], 12, 0);
  G2_RD(W[6], lb1, 38912);
  SCHED();
  G2_MF(5, 1, W[9], 12, 0);
  SCHED();
  G2_MF(5, 0, W[0], 13, 0);
  G2_RD(W[7], lb1, 40960);
  SCHED();
  G2_MF(5, 1, W[0], 13, 0);
  SCHED();
  G2_MF(5, 0, W[1], 14, 0);
  G2_RD(W[8], lb1, 43008);
  SCHED();
  G2_MF(5, 1, W[1], 14, 0);
  G2_XADD(3);
  G2_XADD(4);
  G2_SLAB2(4);
  SCHED();
  G2_MF(5, 0, W[2], 15, 0);
  G2_RD(W[9], lb1, 45056);
  SCHED();
  G2_MF(5, 1, W[2], 15, 0);
  SCHED();
  G2_MF(5, 0, W[3], 16, 0);
  G2_RD(W[0], lb1, 47104);
  SCHED();
  G2_MF(5, 1, W[3], 16, 0);
  SCHED();
  G2_MF(5, 0, W[4], 17, 0);
  G2_RD(W[1], lb1, 49152);
  SCHED();
  G2_MF(5, 1, W[4], 17, 0);
  SCHED();
  WAIT_V(21);
  G2_POLL2(); G2_PLOAD(2); G2_PLOADS(); SCHED();
  G2_MF(6, 0, W[5], 9, 0);
  G2_RD(W[2], lb1, 51200);
  SCHED();
  G2_MF(6, 1, W[5], 9, 0);
  SCHED();
  G2_MF(6, 0, W[6], 10, 0);
  G2_RD(W[3], lb1, 53248);
  SCHED();
  G2_MF(6, 1, W[6], 10, 0);
  SCHED();
  G2_XWRITE(5);
  SCHED();
  G2_MF(6, 0, W[7], 11, 0);
  G2_RD(W[4], lb2, 0);
  SCHED();
  G2_MF(6, 1, W[7], 11, 0);
  SCHED();
  G2_MF(6, 0, W[8], 12, 0);
  G2_RD(W[5], lb2, 2048);
  SCHED();
  G2_MF(6, 1, W[8], 12, 0);
  SCHED();
  G2_MF(6, 0, W[9], 13, 0);
  G2_RD(W[6], lb2, 4096);
  SCHED();
  G2_MF(6, 1, W[9], 13, 0);
  SCHED();
  G2_MF(6, 0, W[0], 14, 0);
  G2_RD(W[7], lb2, 6144);
  SCHED();
  G2_MF(6, 1, W[0], 14, 0);
  SCHED();
  G2_MF(6, 0, W[1], 15, 0);
  G2_RD(W[8], lb2, 8192);
  SCHED();
  G2_MF(6, 1, W[1], 15, 0);
  SCHED();
  G2_MF(6, 0, W[2], 16, 0);
  G2_RD(W[9], lb2, 10240);
  SCHED();
  G2_MF(6, 1, W[2], 16, 0);
  SCHED();
  G2_MF(6, 0, W[3], 17, 0);
  G2_RD(W[0], lb2, 12288);
  SCHED();
  G2_MF(6, 1, W[3], 17, 0);
  SCHED();
  G2_MF(7, 0, W[4], 9, 0);
  G2_RD(W[1], lb2, 14336);
  SCHED();
  G2_MF(7, 1, W[4], 9, 0);
  SCHED();
  G2_MF(7, 0, W[5], 10, 0);
  G2_RD(W[2], lb2, 16384);
  SCHED();
  G2_MF(7, 1, W[5], 10, 0);
  SCHED();
  G2_XWRITE(6);
  SCHED();
  WAIT_VL(18);
  BAR();
  G2_XRD(5);
  G2_XRD(6);
  G2_MF(7, 0, W[6], 11, 0);
  G2_RD(W[3], lb0, 0);
  SCHED();
  G2_MF(7, 1, W[6], 11, 0);
  SCHED();
  G2_MF(7, 0, W[7], 12, 0);
  G2_RD(W[4], lb0, 2048);
  SCHED();
  G2_MF(7, 1, W[7], 12, 0);
  SCHED();
  G2_MF(7, 0, W[8], 13, 0);
  G2_RD(W[5], lb0, 4096);
  SCHED();
  G2_MF(7, 1, W[8], 13, 0);
  SCHED();
  G2_MF(7, 0, W[9], 14, 0);
  G2_RD(W[6], lb0, 6144);
  SCHED();
  G2_MF(7, 1, W[9], 14, 0);
  G2_XADD(5);
  G2_XADD(6);
  G2_SLAB2(6);
  SCHED();
  G2_MF(7, 0, W[0], 15, 0);
  G2_RD(W[7], lb0, 8192);
  SCHED();
  G2_MF(7, 1, W[0], 15, 0);
  SCHED();
  G2_MF(7, 0, W[1], 16, 0);
  G2_RD(W[8], lb0, 10240);
  SCHED();
  G2_MF(7, 1, W[1], 16, 0);
  SCHED();
  G2_PLOAD(1); SCHED();
  G2_MF(7, 0, W[2], 17, 0);
  G2_RD(W[9], lb0, 12288);
  SCHED();
  G2_MF(7, 1, W[2], 17, 0);
  SCHED();
  G2_MF(8, 0, W[3], 9, 0);
  G2_RD(W[0], lb0, 14336);
  SCHED();
  G2_MF(8, 1, W[3], 9, 0);
  SCHED();
  G2_MF(8, 0, W[4], 10, 0);
  G2_RD(W[1], lb0, 16384);
  SCHED();
  G2_MF(8, 1, W[4], 10, 0);
  SCHED();
  G2_XWRITE(7);
  SCHED();
  G2_MF(8, 0, W[5], 11, 0);
  SCHED();
  G2_MF(8, 1, W[5], 11, 0);
  SCHED();
  G2_MF(8, 0, W[6], 12, 0);
  SCHED();
  G2_MF(8, 1, W[6], 12, 0);
  SCHED();
  G2_MF(8, 0, W[7], 13, 0);
  SCHED();
  G2_MF(8, 1, W[7], 13, 0);
  SCHED();
  G2_MF(8, 0, W[8], 14, 0);
  SCHED();
  G2_MF(8, 1, W[8], 14, 0);
  SCHED();
  G2_MF(8, 0, W[9], 15, 0);
  SCHED();
  G2_MF(8, 1, W[9], 15, 0);
  SCHED();
  G2_MF(8, 0, W[0], 16, 0);
  SCHED();
  G2_MF(8, 1, W[0], 16, 0);
  SCHED();
  G2_PLOAD(0); SCHED();
  G2_MF(8, 0, W[1], 17, 0);
  SCHED();
  G2_MF(8, 1, W[1], 17, 0);
  SCHED();
  G2_XWRITE(8);

  asm volatile("s_waitcnt lgkmcnt(0)" ::: "memory");
  BAR();
  G2_XREAD(7);
  G2_XREAD(8);
  {
    unsigned spins = 0;
    while (++spins < (1u << 17)) {
      bool any = false;
#pragma unroll
      for (int o = 0; o < 3; ++o) {
        if (__any(is_sentinel(part[o]))) { part[o] = pload(o); any = true; }
        if (tw == ksp && __any(parts[o][0] == 0xffffffffu || parts[o][1] == 0xffffffffu)) { parts[o] = ploads(o); any = true; }
      }
      if (!any) break;
    }
  }
  asm volatile("s_waitcnt vmcnt(0)"
               : "+v"(res[0]), "+v"(res[1]), "+v"(res[2]), "+v"(res[3]), "+v"(res[4]), "+v"(res[5]), "+v"(res[6]),
                 "+v"(res[7]), "+v"(res[8]), "+v"(res[9]), "+v"(res[10]), "+v"(res[11])::"memory");
#pragma unroll
  for (int it = 0; it < 3; ++it) {
    if (it < 2 || tw == ksp) {
      const f32x4 own = it < 2 ? ac2[7 + it][0] : ac2[0][0];
      f32x4 p[3];
#pragma unroll
      for (int o = 0; o < 3; ++o) {
        const unsigned a0 = it < 2 ? part[o][2 * it] : parts[o][0], a1 = it < 2 ? part[o][2 * it + 1] : parts[o][1];
        p[o] = f32x4{bf_lo(a0), bf_hi(a0), bf_lo(a1), bf_hi(a1)};
      }
      const f32x4 s = (own + p[0]) + (p[1] + p[2]);
      const int n = it < 2 ? nof(7 + it) : 8;
      u32x2 o = {pack_bf(bf_lo(res[4 * it]) + s[0], bf_lo(res[4 * it + 1]) + s[1]),
                 pack_bf(bf_lo(res[4 * it + 2]) + s[2], bf_lo(res[4 * it + 3]) + s[3])};
      *(u32x2*)(out + (size_t)trow * D + d0 + 16 * n + 4 * kq) = o;
    }
  }
  asm volatile("mlp_code_end:");
}

extern "C" void kernel_launch(void* const* d_in, const int* in_sizes, int n_in, void* d_out, int out_size,
                              void* d_ws, size_t ws_size, hipStream_t stream) {
  const bf16_t* x = (const bf16_t*)d_in[0];
  const bf16_t* Are = (const bf16_t*)d_in[1];
  const bf16_t* Aim = (const bf16_t*)d_in[2];
  const bf16_t* Bre = (const bf16_t*)d_in[3];
  const bf16_t* Bim = (const bf16_t*)d_in[4];
  const bf16_t* Cre = (const bf16_t*)d_in[5];
  const bf16_t* Cim = (const bf16_t*)d_in[6];
  const bf16_t* logdt = (const bf16_t*)d_in[7];
  const bf16_t* dskip = (const bf16_t*)d_in[8];
  const bf16_t* w1 = (const bf16_t*)d_in[10];
  const bf16_t* w3 = (const bf16_t*)d_in[11];
  const bf16_t* w2 = (const bf16_t*)d_in[12];
  char* ws = (char*)d_ws;
  (void)hipFuncSetAttribute((const void*)k_mlp, hipFuncAttributeMaxDynamicSharedMemorySize, G2_LDS);
  k_front<<<FB_N, 256, 0, stream>>>(x, Are, Aim, Bre, Bim, Cre, Cim, logdt, dskip, w1, w3, w2, ws);
  k_mlp<<<256, 512, G2_LDS, stream>>>(w1, w3, ws, (bf16_t*)d_out);
}
```
